```python
import torch

def _softmax(x, axis=-1):
    return torch.softmax(x, dim=axis)

def _gelu(x):
    return torch.nn.functional.gelu(x, approximate='tanh')

def _silu(x):
    return torch.nn.functional.silu(x)

def _sigmoid(x):
    return torch.sigmoid(x)

def _layernorm(x, axis=-1, eps=1e-05):
    m = x.mean(dim=axis, keepdim=True)
    v = x.var(dim=axis, keepdim=True, unbiased=False)
    return (x - m) / torch.sqrt(v + eps)

def _scatter_add(tgt, idx, src, axis=0):
    return tgt.clone().index_add_(axis, idx, src)

def _segment_sum(src, seg, n):
    out = torch.zeros((n,) + src.shape[1:], dtype=src.dtype)
    return out.index_add_(0, seg, src)

def _pad(x, pw):
    flat = tuple((v for pair in reversed(pw) for v in pair))
    return torch.nn.functional.pad(x, flat)
kernel_name = 'adaLN_cond__spectral_fnet__geglu_ffn__prompt'

def setup_inputs():

    _w = lambda a: (lambda t: t.to(torch.bfloat16) if t.is_floating_point() else t)(torch.as_tensor(a))

    def _A_setup():
        B, S, D = (1, 2048, 512)
        ns = torch.arange(S)[:, None] * torch.arange(S)[None, :]
        nd = torch.arange(D)[:, None] * torch.arange(D)[None, :]
        Ws_re = torch.cos(-2.0 * torch.pi * ns / S).to(float)
        Ws_im = torch.sin(-2.0 * torch.pi * ns / S).to(float)
        Wd_re = torch.cos(-2.0 * torch.pi * nd / D).to(float)
        Wd_im = torch.sin(-2.0 * torch.pi * nd / D).to(float)
        return {'x': _w((lambda *s: torch.randn(*s, dtype=torch.bfloat16) * 0.05)(B, S, D)), 'dft_seq_re': _w(Ws_re), 'dft_seq_im': _w(Ws_im), 'dft_hid_re': _w(Wd_re), 'dft_hid_im': _w(Wd_im)}
    _A_in = _A_setup()

    def _B_setup():
        B, S, D, F = (1, 2048, 512, 1792)
        return {'x': _w((lambda *s: torch.randn(*s, dtype=torch.bfloat16) * 0.05)(B, S, D)), 'w_gate': _w((lambda *s: torch.randn(*s, dtype=torch.bfloat16) * 0.4)(D, F)), 'w_up': _w((lambda *s: torch.randn(*s, dtype=torch.bfloat16) * 0.4)(D, F)), 'w_down': _w((lambda *s: torch.randn(*s, dtype=torch.bfloat16) * 0.4)(F, D))}
    _B_in = _B_setup()
    return {'A_x': _A_in['x'], 'A_dft_seq_re': _A_in['dft_seq_re'], 'A_dft_seq_im': _A_in['dft_seq_im'], 'A_dft_hid_re': _A_in['dft_hid_re'], 'A_dft_hid_im': _A_in['dft_hid_im'], 'B_x': _B_in['x'], 'B_w_gate': _B_in['w_gate'], 'B_w_up': _B_in['w_up'], 'B_w_down': _B_in['w_down'], 'c': _w((lambda *s: torch.randn(*s, dtype=torch.bfloat16) * 0.2)(1, 512) * 8.0), 'w_mod': _w((lambda *s: torch.randn(*s, dtype=torch.bfloat16) * 0.2)(512, 6 * 512) * 512 ** (-0.5) * 8.0)}

def reference(A_x, A_dft_seq_re, A_dft_seq_im, A_dft_hid_re, A_dft_hid_im, B_x, B_w_gate, B_w_up, B_w_down, c, w_mod):

    def _A_ref(x, dft_seq_re, dft_seq_im, dft_hid_re, dft_hid_im):
        a = torch.einsum('bsd,ed->bse', x, dft_hid_re)
        b = torch.einsum('bsd,ed->bse', x, dft_hid_im)
        y = torch.einsum('ts,bse->bte', dft_seq_re, a) - torch.einsum('ts,bse->bte', dft_seq_im, b)
        return y

    def _B_ref(x, w_gate, w_up, w_down):
        return _gelu(x @ w_gate) * (x @ w_up) @ w_down
    x = A_x
    mod = (_silu(c) @ w_mod).reshape(c.shape[0], 6, 1, -1)
    sa, ca, ga, sb, cb, gb = (mod[:, 0].clone(), mod[:, 1].clone(), mod[:, 2].clone(), mod[:, 3].clone(), mod[:, 4].clone(), mod[:, 5].clone())
    h = _layernorm(x, axis=-1) * (1 + ca) + sa
    x = x + ga * _A_ref(h, A_dft_seq_re, A_dft_seq_im, A_dft_hid_re, A_dft_hid_im)
    h = _layernorm(x, axis=-1) * (1 + cb) + sb
    x = x + gb * _B_ref(h, B_w_gate, B_w_up, B_w_down)
    return x
```

```cpp
#include <hip/hip_runtime.h>
#include <hip/hip_bf16.h>
typedef unsigned short u16;
__device__ __forceinline__ float bf2f(u16 v){ return __uint_as_float(((unsigned)v)<<16); }
__device__ __forceinline__ u16 f2bf(float f){ __hip_bfloat16 b=__float2bfloat16(f); return *(u16*)&b; }
constexpr int S=2048,D=512,F=1792;
__global__ void k_mod(const u16* c,const u16* w,float* mod){ int j=blockIdx.x*blockDim.x+threadIdx.x; float a=0; for(int k=0;k<D;++k){ float cv=bf2f(c[k]); float s=cv/(1.f+__expf(-cv)); s=bf2f(f2bf(s)); a+=s*bf2f(w[k*3072+j]);} mod[j]=a; }
__global__ void k_ln1(const u16* x,u16* n){ int s=blockIdx.x; __shared__ float r[2][64]; int t=threadIdx.x; float v[8],a=0; for(int i=0;i<8;++i){v[i]=bf2f(x[s*D+t*8+i]);a+=v[i];} for(int o=32;o;o>>=1)a+=__shfl_xor(a,o); float mu=a/D,q=0; for(int i=0;i<8;++i){v[i]-=mu;q+=v[i]*v[i];} for(int o=32;o;o>>=1)q+=__shfl_xor(q,o); float rs=rsqrtf(q/D+1e-5f); for(int i=0;i<8;++i)n[s*D+t*8+i]=f2bf(v[i]*rs);}
__global__ void k_pq(const u16* wre,const u16* wim,const u16* n,const float* mod,u16* pq){ int d=blockIdx.x*64+(threadIdx.x&63); int t=blockIdx.y*4+(threadIdx.x>>6); float p=0,q=0,rp=0,rq=0; for(int s=0;s<S;++s){ float nv=bf2f(n[s*D+d]); float a=bf2f(wre[t*S+s]),b=bf2f(wim[t*S+s]); p+=a*nv;q+=b*nv;rp+=a;rq+=b;} float ca=1.f+mod[512+d], sa=mod[d]; pq[t*1024+d]=f2bf(p*ca+rp*sa); pq[t*1024+512+d]=f2bf(-(q*ca+rq*sa)); }
__global__ void k_y(const u16* pq,const u16* wdre,const u16* wdim,const u16* x,const float* mod,float* x1,u16* ap){ int e=blockIdx.x*64+(threadIdx.x&63); int t=blockIdx.y*4+(threadIdx.x>>6); float y=0; for(int d=0;d<D;++d){ y+=bf2f(pq[t*1024+d])*bf2f(wdre[e*D+d])+bf2f(pq[t*1024+512+d])*bf2f(wdim[e*D+d]);} float v=bf2f(x[t*D+e])+mod[1024+e]*y; x1[t*D+e]=v; ap[t*D+e]=f2bf(v*(1.f+mod[2048+e])); }
__global__ void k_stats(const float* x1,float* st){ int s=blockIdx.x; int t=threadIdx.x; float v[8],a=0; for(int i=0;i<8;++i){v[i]=x1[s*D+t*8+i];a+=v[i];} for(int o=32;o;o>>=1)a+=__shfl_xor(a,o); float mu=a/D,q=0; for(int i=0;i<8;++i){v[i]-=mu;q+=v[i]*v[i];} for(int o=32;o;o>>=1)q+=__shfl_xor(q,o); if(t==0){st[2*s]=mu; st[2*s+1]=rsqrtf(q/D+1e-5f);} }
__global__ void k_uv(const u16* wg,const u16* wu,const float* mod,float* uv){ int f=blockIdx.x*64+threadIdx.x; float ug=0,vg=0,uu=0,vu=0; for(int d=0;d<D;++d){ float c=1.f+mod[2048+d], s=mod[1536+d]; float g=bf2f(wg[d*F+f]),u=bf2f(wu[d*F+f]); ug+=c*g;vg+=s*g;uu+=c*u;vu+=s*u;} uv[f]=ug;uv[F+f]=vg;uv[2*F+f]=uu;uv[3*F+f]=vu; }
__global__ void k_ffn1(const u16* ap,const u16* wg,const u16* wu,const float* st,const float* uv,u16* g){ int f=blockIdx.x*64+(threadIdx.x&63); int t=blockIdx.y*4+(threadIdx.x>>6); float a=0,b=0; for(int d=0;d<D;++d){ float h=bf2f(ap[t*D+d]); a+=h*bf2f(wg[d*F+f]); b+=h*bf2f(wu[d*F+f]);} float mu=st[2*t],rs=st[2*t+1]; a=rs*(a-mu*uv[f])+uv[F+f]; b=rs*(b-mu*uv[2*F+f])+uv[3*F+f]; float z=0.7978845608f*(a+0.044715f*a*a*a); float ge=a/(1.f+__expf(-2.f*z)); g[t*F+f]=f2bf(ge*b); }
__global__ void k_ffn2(const u16* g,const u16* wd,const float* x1,const float* mod,u16* out){ int e=blockIdx.x*64+(threadIdx.x&63); int t=blockIdx.y*4+(threadIdx.x>>6); float a=0; for(int f=0;f<F;++f) a+=bf2f(g[t*F+f])*bf2f(wd[f*D+e]); out[t*D+e]=f2bf(x1[t*D+e]+mod[2560+e]*a); }
extern "C" void kernel_launch(void* const* d_in, const int* in_sizes, int n_in, void* d_out, int out_size, void* d_ws, size_t ws_size, hipStream_t stream) {
  const u16* x=(const u16*)d_in[0],*wre=(const u16*)d_in[1],*wim=(const u16*)d_in[2],*wdre=(const u16*)d_in[3],*wdim=(const u16*)d_in[4];
  const u16* wg=(const u16*)d_in[6],*wu=(const u16*)d_in[7],*wd=(const u16*)d_in[8],*c=(const u16*)d_in[9],*wm=(const u16*)d_in[10];
  char* ws=(char*)d_ws; float* mod=(float*)ws; float* st=(float*)(ws+65536); float* uv=(float*)(ws+131072); u16* n=(u16*)(ws+(1<<20)); u16* pq=(u16*)(ws+(4<<20)); float* x1=(float*)(ws+(8<<20)); u16* ap=(u16*)(ws+(12<<20)); u16* g=(u16*)(ws+(16<<20));
  k_mod<<<12,256,0,stream>>>(c,wm,mod);
  k_ln1<<<S,64,0,stream>>>(x,n);
  k_pq<<<dim3(8,512),256,0,stream>>>(wre,wim,n,mod,pq);
  k_y<<<dim3(8,512),256,0,stream>>>(pq,wdre,wdim,x,mod,x1,ap);
  k_stats<<<S,64,0,stream>>>(x1,st);
  k_uv<<<28,64,0,stream>>>(wg,wu,mod,uv);
  k_ffn1<<<dim3(28,512),256,0,stream>>>(ap,wg,wu,st,uv,g);
  k_ffn2<<<dim3(8,512),256,0,stream>>>(g,wd,x1,mod,(u16*)d_out);
}
```

```cpp
#include <hip/hip_runtime.h>

typedef unsigned short u16;
typedef unsigned long long u64;
typedef short bf16x8 __attribute__((ext_vector_type(8)));
typedef short s16x4 __attribute__((ext_vector_type(4)));
typedef __bf16 bf2 __attribute__((ext_vector_type(2)));
typedef float f32x16 __attribute__((ext_vector_type(16)));
typedef float f32x4 __attribute__((ext_vector_type(4)));
typedef float f32x2 __attribute__((ext_vector_type(2)));
typedef unsigned u32x4 __attribute__((ext_vector_type(4)));
typedef unsigned u32x2 __attribute__((ext_vector_type(2)));
#define DI __device__ __forceinline__
#define LAS __attribute__((address_space(3)))
#define GAS __attribute__((address_space(1)))

constexpr unsigned WS_NONCE = 0;
constexpr unsigned WS_NFLAG = 98304 + 16384;
constexpr unsigned WS_MODFLAG = 4096;
constexpr unsigned WS_AUXFLAG = 8192;
constexpr unsigned WS_CPQ = 16384;
constexpr unsigned WS_CAP = 32768;
constexpr unsigned WS_CG = 49152;
constexpr unsigned WS_MODP = 65536;
constexpr unsigned WS_UVP = 131072;
constexpr unsigned WS_STATS = 393216;
constexpr unsigned WS_NT = 1u << 20;
constexpr unsigned WS_PQ = 4u << 20;
constexpr unsigned WS_AP = 8u << 20;
constexpr unsigned WS_G = 10u << 20;
constexpr unsigned WS_WT = 18u << 20;
constexpr unsigned WS_WDT = 22u << 20;
constexpr unsigned WS_DIAG = 24u << 20;
constexpr unsigned WS_BYTES = 26u << 20;

constexpr unsigned L_STAGE = 24576;
constexpr unsigned L_AUX = 98304;
constexpr unsigned L_LN = 122880;
constexpr unsigned L_HK = 132096;
constexpr unsigned L_TAB = 155648;
constexpr unsigned L_MOD = L_TAB;
constexpr unsigned L_VB = L_TAB + 1280;
constexpr unsigned L_UV = L_TAB + 1536;
constexpr unsigned L_ST = L_TAB + 3328;
constexpr unsigned L_SY = L_TAB + 4352;
constexpr unsigned LDS_BYTES = 163840;
enum { SY_ADONE = 0, SY_STAT = 4, SY_UFIN = 5, SY_GSIG = 9, SY_WL = 10, SY_PROG = 14, SY_LN = 16, SY_ND = 17, SY_WD = 20, SY_N = 24, SY_EP = 24, SY_BAP = 28 };
constexpr unsigned L_JUNK = L_TAB + 6144;
constexpr int W_PACE = 4;

constexpr int SPIN_CAP = 1 << 20;
constexpr int CODE_LINES_PER_WG = 8;

struct Params {
  const u16 *x, *wsre, *wsim, *wdre, *wdim, *wg, *wu, *wdn, *c, *wmod;
  u16* out;
  char* ws;
};

#if defined(__HIP_DEVICE_COMPILE__)
#pragma clang attribute push(__attribute__((target("no-packed-fp32-ops"))), apply_to = function)
#endif
DI float u2f(unsigned u) { return __builtin_bit_cast(float, u); }
DI unsigned f2u(float f) { return __builtin_bit_cast(unsigned, f); }
DI bool all_lanes(bool c) { return __builtin_amdgcn_ballot_w64(c) == ~0ull; }
DI u32x4 mk_srd(const void* p, unsigned bytes) {
  u64 a = (u64)p;
  u32x4 r = {(unsigned)a, (unsigned)(a >> 32) & 0xffffu, bytes, 0x00020000u};
  return r;
}
DI __amdgpu_buffer_rsrc_t mk_rsrc(const void* p, unsigned bytes) {
  return __builtin_amdgcn_make_buffer_rsrc((void*)p, 0, bytes, 0x00020000);
}
#define RFL(x) __builtin_amdgcn_readfirstlane(x)
#define BAR() __builtin_amdgcn_s_barrier()
#define WAITV(n) asm volatile("s_waitcnt vmcnt(%0)" ::"i"(n) : "memory")
#define WAITL0() __builtin_amdgcn_s_waitcnt(0xC07F)
#define CFENCE() asm volatile("" ::: "memory")

DI void dma4v(u32x4 srd, unsigned v0, unsigned v1, unsigned v2, unsigned v3, unsigned soff, unsigned m0v) {
  asm volatile("s_mov_b32 m0, %0\n\ts_nop 0\n\t"
               "buffer_load_dwordx4 %1, %5, %6 offen lds\n\t"
               "buffer_load_dwordx4 %2, %5, %6 offen offset:1024 lds\n\t"
               "buffer_load_dwordx4 %3, %5, %6 offen offset:2048 lds\n\t"
               "buffer_load_dwordx4 %4, %5, %6 offen offset:3072 lds"
               ::"s"(m0v), "v"(v0), "v"(v1), "v"(v2), "v"(v3), "s"(srd), "s"(soff) : "memory");
}
DI void dma2v(u32x4 srd, unsigned v0, unsigned v1, unsigned soff, unsigned m0v) {
  asm volatile("s_mov_b32 m0, %0\n\ts_nop 0\n\t"
               "buffer_load_dwordx4 %1, %3, %4 offen lds\n\t"
               "buffer_load_dwordx4 %2, %3, %4 offen offset:1024 lds"
               ::"s"(m0v), "v"(v0), "v"(v1), "s"(srd), "s"(soff) : "memory");
}
DI void dma2c(u32x4 srd, unsigned v, unsigned soff, unsigned m0v) {
  asm volatile("s_mov_b32 m0, %0\n\ts_nop 0\n\t"
               "buffer_load_dwordx4 %1, %2, %3 offen sc1 lds\n\t"
               "buffer_load_dwordx4 %1, %2, %3 offen offset:1024 sc1 lds"
               ::"s"(m0v), "v"(v), "s"(srd), "s"(soff) : "memory");
}
DI void dma4c(u32x4 srd, unsigned v, unsigned soff, unsigned m0v) {
  asm volatile("s_mov_b32 m0, %0\n\ts_nop 0\n\t"
               "buffer_load_dwordx4 %1, %2, %3 offen sc1 lds\n\t"
               "buffer_load_dwordx4 %1, %2, %3 offen offset:1024 sc1 lds\n\t"
               "buffer_load_dwordx4 %1, %2, %3 offen offset:2048 sc1 lds\n\t"
               "buffer_load_dwordx4 %1, %2, %3 offen offset:3072 sc1 lds"
               ::"s"(m0v), "v"(v), "s"(srd), "s"(soff) : "memory");
}
DI void dma1w(u32x4 srd, unsigned v, unsigned soff, unsigned m0v) {
  asm volatile("s_mov_b32 m0, %0\n\ts_nop 0\n\tbuffer_load_dword %1, %2, %3 offen sc1 lds" ::"s"(m0v), "v"(v), "s"(srd), "s"(soff) : "memory");
}
DI unsigned opv(unsigned v) { asm volatile("" : "+v"(v)); return v; }
DI int ops(int v) { asm volatile("" : "+s"(v)); return v; }
#define SCHED() __builtin_amdgcn_sched_barrier(0)
#define SGB(m, n) __builtin_amdgcn_sched_group_barrier((m), (n), 0)
DI bf16x8 lds_frag(unsigned a) { return *(const LAS bf16x8*)a; }
DI f32x4 lds_f4(unsigned a) { return *(const LAS f32x4*)a; }
DI bf16x8 lds_tr2(unsigned a0, unsigned a1) {
  s16x4 lo = __builtin_amdgcn_ds_read_tr16_b64_v4i16((LAS s16x4*)a0);
  s16x4 hi = __builtin_amdgcn_ds_read_tr16_b64_v4i16((LAS s16x4*)a1);
  return __builtin_shufflevector(lo, hi, 0, 1, 2, 3, 4, 5, 6, 7);
}
DI unsigned pk2(float a, float b) { bf2 p = {(__bf16)a, (__bf16)b}; return __builtin_bit_cast(unsigned, p); }
DI float bflo(unsigned u) { return u2f(u << 16); }
DI float bfhi(unsigned u) { return u2f(u & 0xffff0000u); }
DI float rbf(float a) { return (float)(__bf16)a; }
DI float sum32(float v) { auto r = __builtin_amdgcn_permlane32_swap(f2u(v), f2u(v), false, false); return u2f(r[0]) + u2f(r[1]); }
DI float sum16(float v) { auto r = __builtin_amdgcn_permlane16_swap(f2u(v), f2u(v), false, false); return u2f(r[0]) + u2f(r[1]); }
template <int CTRL> DI float dpp_add(float v) { return v + u2f(__builtin_amdgcn_update_dpp(0u, f2u(v), CTRL, 0xf, 0xf, true)); }
DI float wave_sum(float v) {
  v = dpp_add<0xB1>(v);
  v = dpp_add<0x4E>(v);
  v = dpp_add<0x141>(v);
  v = dpp_add<0x140>(v);
  return sum32(sum16(v));
}
#define MFMA32(a, b, c) __builtin_amdgcn_mfma_f32_32x32x16_bf16((a), (b), (c), 0, 0, 0)
#define MFMA16(a, b, c) __builtin_amdgcn_mfma_f32_16x16x32_bf16((a), (b), (c), 0, 0, 0)

DI unsigned atomic_peek(const char* p) {
  unsigned r, z = 0;
  asm volatile("global_atomic_add %0, %1, %2, off sc0\n\ts_waitcnt vmcnt(0)" : "=&v"(r) : "v"(p), "v"(z) : "memory");
  return r;
}
DI void cnt_add(char* p) { asm volatile("global_atomic_add %0, %1, off" ::"v"(p), "v"(1u) : "memory"); }
DI unsigned flag_ld(const char* p) { return __hip_atomic_load((GAS unsigned*)p, __ATOMIC_RELAXED, __HIP_MEMORY_SCOPE_AGENT); }
DI void flag_st(char* p, unsigned v) { asm volatile("global_atomic_swap %0, %1, off" ::"v"(p), "v"(v) : "memory"); }
DI void cnt_wait(const char* p, unsigned base, int lane, int nl) {
_Pragma("unroll 1") for (int sp = 0; sp < SPIN_CAP; ++sp) {
    unsigned v = base + 16u;
    if (lane < nl) v = atomic_peek(p);
    if (all_lanes(v - base >= 16u)) break;
  }
  CFENCE();
}
DI void lds_wait_ge(unsigned word, unsigned want) {
  volatile LAS unsigned* p = (volatile LAS unsigned*)(L_SY + word * 4);
_Pragma("unroll 1") for (int sp = 0; sp < SPIN_CAP; ++sp) {
    if (RFL(*p) >= want) break;
    __builtin_amdgcn_s_sleep(1);
  }
  CFENCE();
}
DI void lds_wait4(unsigned word) {
  volatile LAS u32x4* p = (volatile LAS u32x4*)(L_SY + word * 4);
_Pragma("unroll 1") for (int sp = 0; sp < SPIN_CAP; ++sp) {
    const u32x4 v = *p;
    if (RFL(v[0] & v[1] & v[2] & v[3])) break;
    __builtin_amdgcn_s_sleep(1);
  }
  CFENCE();
}
DI unsigned lds_get(unsigned word) { return *(volatile LAS unsigned*)(L_SY + word * 4); }
DI void lds_set(unsigned word, unsigned v) { *(volatile LAS unsigned*)(L_SY + word * 4) = v; }
DI unsigned lds_inc(unsigned word) { return __hip_atomic_fetch_add((LAS unsigned*)(L_SY + word * 4), 1u, __ATOMIC_RELAXED, __HIP_MEMORY_SCOPE_WORKGROUP); }

#define STAMP(i)

DI void load_w(bf16x8 (&W)[32], u32x4 (&uv)[2], __amdgpu_buffer_rsrc_t rws, int c, int rot, unsigned lane) {
  const unsigned uo = (lane >> 4) * 28672 + (lane & 15) * 16;
  uv[0] = __builtin_amdgcn_raw_buffer_load_b128(rws, uo, WS_UVP + (unsigned)c * 256u, 16);
  uv[1] = __builtin_amdgcn_raw_buffer_load_b128(rws, uo, WS_UVP + (unsigned)(448 + c) * 256u, 16);
  const unsigned vl = opv(lane * 16);
#pragma unroll
  for (int k = 0; k < 32; ++k) {
    W[k] = __builtin_bit_cast(bf16x8, __builtin_amdgcn_raw_buffer_load_b128(rws, vl + (k & 3) * 1024, WS_WT + (unsigned)(c * 32 + (((k >> 2) + rot) & 7) * 4) * 1024u, 16));
    if ((k & 1) == 1 && k >= W_PACE && k < 31) WAITV(W_PACE);
  }
  SCHED();
}

DI void k4_body(const bf16x8 (&W)[32], const u32x4 (&uv)[2], __amdgpu_buffer_rsrc_t rws, char* ws, int wv, unsigned lane, int tp4, int ft, int rot) {
  const unsigned h = lane >> 5, t = lane & 31;
  const int c = 7 * ft + wv;
  {
    f32x4 a;
#pragma unroll
    for (int k = 0; k < 4; ++k) a[k] = sum32(sum16(u2f(uv[0][k]) + u2f(uv[1][k])));
    if (lane < 16) *(LAS f32x4*)(L_UV + wv * 256 + lane * 16) = a;
  }
  const unsigned gvoff = (t >> 4) * 57344 + h * 256 + (t & 15) * 16 + (c >> 1) * 1024 + (c & 1) * 512;
  const unsigned ub = opv(L_UV + wv * 256 + h * 128);
  const unsigned sb = opv(L_ST + t * 8);
#pragma unroll 1
  for (int u = 0; u < 4; ++u) {
    const int tbl = (u + ft) & 3;
    lds_wait_ge(SY_ADONE + u, 1);
    STAMP(16 + 2 * u);
    f32x16 acc = {};
    bf16x8 A[32];
    unsigned ab[8];
#pragma unroll
    for (int g = 0; g < 8; ++g) ab[g] = opv(lane * 16 + (unsigned)(u * 32768 + ((g + rot) & 7) * 4096));
    SCHED();
#pragma unroll
    for (int k = 0; k < 32; ++k) A[k] = lds_frag(ab[k >> 2] + (k & 3) * 1024);
#pragma unroll
    for (int k = 0; k < 32; ++k) acc = MFMA32(W[k], A[k], acc);
    SGB(0x100, 6);
#pragma unroll
    for (int k = 0; k < 26; ++k) { SGB(0x8, 1); SGB(0x100, 1); }
    SGB(0x8, 6);
    SCHED();
    STAMP(17 + 2 * u);
    if (lane == 0) lds_inc(SY_UFIN + u);
    if (u == 0) lds_wait_ge(SY_STAT, 1);
    const f32x2 st = *(const LAS f32x2*)(sb + tbl * 256);
    f32x4 ug[2], uu[2], vg[2], vu[2];
#pragma unroll
    for (int g = 0; g < 2; ++g) { ug[g] = lds_f4(ub + g * 16); uu[g] = lds_f4(ub + 32 + g * 16); vg[g] = lds_f4(ub + 64 + g * 16); vu[g] = lds_f4(ub + 96 + g * 16); }
    SCHED();
    float o[8];
#pragma unroll
    for (int g = 0; g < 2; ++g) {
#pragma unroll
      for (int j = 0; j < 4; ++j) {
        const float a = fmaf(acc[4 * g + j], st[0], fmaf(st[1], ug[g][j], vg[g][j]));
        const float b = fmaf(acc[8 + 4 * g + j], st[0], fmaf(st[1], uu[g][j], vu[g][j]));
        const float e = a * fmaf(a * a, -0.10294324f, -2.30220820f);
        const float s = __builtin_amdgcn_rcpf(1.f + __builtin_amdgcn_exp2f(e));
        o[4 * g + j] = a * b * s;
      }
    }
    const u32x4 pk = {pk2(o[0], o[1]), pk2(o[2], o[3]), pk2(o[4], o[5]), pk2(o[6], o[7])};
    __builtin_amdgcn_raw_buffer_store_b128(pk, rws, gvoff, WS_G + (unsigned)(2 * (4 * tp4 + tbl)) * 57344u, 16);
  }
  WAITV(0);
  unsigned old = 0;
  if (lane == 0) old = lds_inc(SY_GSIG);
  if (RFL(old) == 6 && lane < 4) cnt_add(ws + WS_CG + (4 * tp4 + lane) * 256);
}

#define C_XCD(b) ((b) & 7)
#define C_LOC(b) ((b) >> 3)
#define C_TP(b) (2 * C_XCD(b) + (C_LOC(b) >> 4))
#define C_PART(b) ((C_LOC(b) >> 3) & 1)
#define C_DN(b) (C_LOC(b) & 7)
#define C_J0(b) (4 * ((C_DN(b) + C_XCD(b) + 4 * C_PART(b)) & 7))
#define C_TB64(b) (4 * C_XCD(b) + (C_LOC(b) >> 3))
#define C_TP4(b) (8 * (C_XCD(b) & 1) + (C_LOC(b) >> 2))
#define C_FT(b) (4 * (C_XCD(b) >> 1) + (C_LOC(b) & 3))
#define C_ROT5(b) ((7 * C_DN(b) / 2 + 7 * (C_TB64(b) & 3)) % 28)

__global__ __launch_bounds__(512, 2) void fused_block(Params p) {
  const int b0 = blockIdx.x;
  const unsigned lane0 = threadIdx.x & 63;
  const int wv = RFL(threadIdx.x >> 6);
  char* const ws = p.ws;
  u64 pc0;
  asm volatile("s_getpc_b64 %0" : "=s"(pc0));
  STAMP(0);
  const u32x4 sws = mk_srd(ws, WS_BYTES);
  const __amdgpu_buffer_rsrc_t rws = mk_rsrc(ws, WS_BYTES);

  if (wv >= 4) {
    const int wl = wv - 4;
    const unsigned lane = lane0;
    const unsigned sw = (lane & 7) ^ (lane >> 4);
    const unsigned vl = lane * 16;
    {
      const int b = b0, j0 = C_J0(b), dn = C_DN(b);
      unsigned junk = 0;
      if (wv == 4 && lane < CODE_LINES_PER_WG)
        asm volatile("global_load_dword %0, %1, off" : "=&v"(junk) : "v"((const char*)(pc0 & ~255ull) + (C_LOC(b) * CODE_LINES_PER_WG + lane) * 128) : "memory");
      const u32x4 sA = mk_srd(C_PART(b) ? p.wsim : p.wsre, 2048u * 2048u * 2u);
      const unsigned ra = (unsigned)(128 * C_TP(b) + 32 * wl + (lane >> 3)) * 4096u;
      const unsigned va0 = ra + sw * 16, va1 = ra + 8 * 4096 + (sw ^ 4) * 16 - 1024, va2 = ra + 16 * 4096 + sw * 16 - 2048, va3 = ra + 24 * 4096 + (sw ^ 4) * 16 - 3072;
      dma2v(sA, va0, va1, (unsigned)j0 * 128u, wl * 4096);
      BAR();
      dma2v(sA, va2 + 2048, va3 + 2048, (unsigned)j0 * 128u, wl * 4096 + 2048);
      dma2v(sA, va0, va1, (unsigned)(j0 + 1) * 128u, L_STAGE + wl * 4096);
      asm volatile("s_waitcnt vmcnt(4)" : "+v"(junk)::"memory");
      dma2v(sA, va2 + 2048, va3 + 2048, (unsigned)(j0 + 1) * 128u, L_STAGE + wl * 4096 + 2048);
#pragma unroll
      for (int s = 2; s < 4; ++s) {
        WAITV(4);
        dma2v(sA, va0, va1, (unsigned)(j0 + s) * 128u, s * L_STAGE + wl * 4096);
        WAITV(4);
        dma2v(sA, va2 + 2048, va3 + 2048, (unsigned)(j0 + s) * 128u, s * L_STAGE + wl * 4096 + 2048);
      }
      {
        const unsigned m0 = (unsigned)(128 * (b & 3) + 32 * wl + (lane >> 3)) * 6144u + (lane & 7) * 16 + (b < 192 ? (b >> 2) * 128 : 0x40000000);
        WAITV(2);
        dma4v(mk_srd(p.wmod, 512u * 3072u * 2u), m0, m0 + 8 * 6144 - 1024, m0 + 16 * 6144 - 2048, m0 + 24 * 6144 - 3072, 0, L_AUX + wl * 4096);
      }
      STAMP(1);
      BAR();
      STAMP(2);
      BAR();
      auto stage = [&](int s) {
        const unsigned j = (unsigned)(j0 + s) & 31u, sl = (unsigned)(s & 3) * L_STAGE;
        dma4v(sA, va0, va1, va2, va3, j * 128u, sl + wl * 4096);
        dma2c(sws, vl, WS_NT + ((unsigned)dn * 32u + j) * 8192u + wl * 2048, sl + 16384 + wl * 2048);
      };
      stage(4);
      BAR();
      stage(5);
      WAITV(16); BAR();
#pragma unroll 1
      for (int s = 4; s <= 28; ++s) {
        stage(s + 2); WAITV(12); BAR(); if ((s & 3) == 0) STAMP(23 + (s >> 2)); }
      stage(31);
    }
    const int b = ops(b0);
    {
      u32x4 f0;
      const char* fp = ws + WS_AUXFLAG + (lane < 56 ? lane : 55u) * 16;
      asm volatile("global_load_dwordx4 %0, %1, off sc1" : "=&v"(f0) : "v"(fp) : "memory");
      const unsigned rd = (unsigned)(64 * C_DN(b) + 16 * wl + (lane >> 3)) * 1024u;
      const unsigned vd0 = rd + sw * 16, vd1 = rd + 8 * 1024 + (sw ^ 4) * 16 - 1024;
      auto wdblk = [&](int n) {
        const unsigned blk = (unsigned)(n + 4 * (C_TB64(b) & 3)) & 15u;
        const unsigned sl = (unsigned)(n < 7 ? 12 + n : n - 7) * 8192u + wl * 2048;
        dma2v(mk_srd(blk >= 8 ? p.wdim : p.wdre, 512u * 512u * 2u), vd0, vd1, (blk & 7u) * 128u, sl);
      };
      WAITV(13); BAR();
      WAITV(7); BAR();
      wdblk(0);
      asm volatile("s_waitcnt vmcnt(2)" : "+v"(f0)::"memory");
      BAR();
      STAMP(3);
      const unsigned ep = RFL(lds_get(SY_EP));
      if (!all_lanes(f0[0] == ep && f0[1] == ep && f0[2] == ep && f0[3] == ep)) {
_Pragma("unroll 1") for (int sp = 0; sp < SPIN_CAP; ++sp) {
          const unsigned a0 = flag_ld(fp), a1 = flag_ld(fp + 4), a2 = flag_ld(fp + 8), a3 = flag_ld(fp + 12);
          if (all_lanes(a0 == ep && a1 == ep && a2 == ep && a3 == ep)) break;
        }
      }
      CFENCE();
      STAMP(9);
      {
        const unsigned li = wl * 56 + (lane < 56 ? lane : 55u);
        dma1w(sws, (li >> 5) * 32768 + (li & 31) * 128, WS_WT + (unsigned)(7 * C_FT(b) * 32 + (C_LOC(b) >> 2) * 4) * 1024u, L_JUNK);
      }
      wdblk(1);
#pragma unroll 1
      for (int n = 2; n < 16; ++n) { wdblk(n); WAITV(4); if ((n & 3) == 2) STAMP(10 + (n >> 2)); }
    }
    const int tp4 = C_TP4(b), ft = C_FT(b), rot4 = C_LOC(b) >> 2;
    if (wv < 7) {
      bf16x8 W[32];
      u32x4 uv[2];
      WAITV(0);
      lds_set(SY_WD + wl, 1);
      STAMP(4);
      if (wv != 4) BAR();
      load_w(W, uv, rws, 7 * ft + wv, rot4, lane);
      if (wv == 4) BAR();
      STAMP(5);
      k4_body(W, uv, rws, ws, wv, lane, tp4, ft, rot4);
      STAMP(8);
    } else {
      WAITV(0);
      lds_set(SY_WD + 3, 1);
      STAMP(4);
      const unsigned baseAP = lds_get(SY_BAP + (lane & 3));
      BAR();
      STAMP(5);
      const int rot5 = C_ROT5(b), et = C_DN(b);
      auto wdn = [&](int n0, int n1) {
#pragma unroll 1
        for (int n = n0; n < n1; ++n) {
          int kb = n + rot5; kb = kb >= 28 ? kb - 28 : kb;
          int sn = n + 16; sn = sn >= 38 ? sn - 38 : sn >= 19 ? sn - 19 : sn;
          const unsigned so = WS_WDT + (unsigned)(et * 28 + kb) * 8192u, sl = (unsigned)sn * 8192u;
          dma4c(sws, vl, so, sl); dma4c(sws, vl, so + 4096, sl + 4096);
          WAITV(8);
        }
      };
      cnt_wait(ws + WS_CAP + (4 * tp4 + lane) * 256, baseAP, lane, 4);
      STAMP(6);
      auto ablk = [&](int h0, int h1) {
#pragma unroll 1
        for (int hh = h0; hh < h1; ++hh) {
          const unsigned so = WS_AP + (unsigned)(4 * tp4 + (((hh >> 1) + ft) & 3)) * 32768u + (hh & 1) * 16384, sl = (unsigned)hh * 16384u;
#pragma unroll
          for (int q = 0; q < 4; ++q) dma4c(sws, vl, so + q * 4096, sl + q * 4096);
        }
      };
      ablk(0, 3);
      WAITV(16); lds_set(SY_ADONE + 0, 1);
      STAMP(7);
      ablk(3, 4);
      u32x2 sp[2][8];
#pragma unroll
      for (int hf = 0; hf < 2; ++hf)
#pragma unroll
        for (int e = 0; e < 8; ++e)
          sp[hf][e] = __builtin_amdgcn_raw_buffer_load_b64(rws, lane * 8 + hf * 512, WS_STATS + (unsigned)(e * 2048 + 128 * tp4) * 8u, 16);
      CFENCE();
      WAITV(16); lds_set(SY_ADONE + 1, 1);
      STAMP(16);
#pragma unroll
      for (int hf = 0; hf < 2; ++hf) {
        float mu = 0.f, m2 = 0.f;
#pragma unroll
        for (int e = 0; e < 8; ++e) { mu += u2f(sp[hf][e][0]); m2 += u2f(sp[hf][e][1]); }
        mu *= 0.125f;
        float dv = 0.f;
#pragma unroll
        for (int e = 0; e < 8; ++e) { const float d = u2f(sp[hf][e][0]) - mu; dv = fmaf(d, d, dv); }
        const float rstd = __builtin_amdgcn_rsqf(fmaf(dv, 64.f, m2) * (1.f / 512.f) + 1e-5f);
        const f32x2 o = {rstd, -mu * rstd};
        *(LAS f32x2*)(L_ST + (64 * hf + lane) * 8) = o;
      }
      WAITL0(); lds_set(SY_STAT, 1);
      STAMP(17);
      ablk(4, 7);
      WAITV(16); lds_set(SY_ADONE + 2, 1);
      STAMP(18);
      ablk(7, 8);
      WAITV(0); lds_set(SY_ADONE + 3, 1);
      STAMP(19);
      wdn(0, 3);
      lds_wait_ge(SY_UFIN + 0, 7); wdn(3, 7);
      lds_wait_ge(SY_UFIN + 1, 7); wdn(7, 11);
      lds_set(SY_WL + 0, 1);
      STAMP(20);
      lds_wait_ge(SY_UFIN + 2, 7); wdn(11, 15);
      lds_set(SY_WL + 1, 1);
      lds_wait_ge(SY_UFIN + 3, 7); wdn(15, 19);
      STAMP(8);
      lds_wait_ge(SY_PROG + 0, 4);
      wdn(19, 22);
      lds_set(SY_WL + 2, 1);
      STAMP(21);
      wdn(22, 26);
      lds_wait_ge(SY_PROG + 1, 4);
      wdn(26, 28);
      WAITV(0); lds_set(SY_WL + 3, 1);
      STAMP(22);
    }
  } else {
    const int w = wv;
    unsigned epoch, basePQ, baseG;
    {
      const int b = b0;
      const unsigned lane = lane0, h = lane >> 5, r32 = lane & 31, q = lane >> 4, r16 = lane & 15;
      u32x4 xa, xb;
      asm volatile("global_load_dwordx4 %0, %2, off\n\tglobal_load_dwordx4 %1, %2, off offset:1024" : "=&v"(xa), "=&v"(xb) : "v"((const char*)p.x + (8 * b + 2 * w) * 1024 + lane * 16) : "memory");
      unsigned sv = 0, ev;
      asm volatile("global_load_dword %0, %1, off sc1" : "=&v"(ev) : "v"(ws + WS_NONCE) : "memory");
      {
        const unsigned off = lane < 2 ? (lane ? WS_CG : WS_CPQ) + (2 * C_TB64(b) + (w >> 1)) * 256 : WS_CAP + (4 * C_TP4(b) + lane - 2) * 256;
        if (lane < (w == 0 ? 6u : 2u)) asm volatile("global_atomic_add %0, %1, %2, off sc0" : "=&v"(sv) : "v"(ws + off), "v"(0u) : "memory");
      }
      if (threadIdx.x < SY_N) lds_set(threadIdx.x, 0);
      WAITL0(); BAR();
      asm volatile("s_waitcnt vmcnt(2)" : "+v"(xa), "+v"(xb)::"memory");
      STAMP(13);
      {
        float va[8], vb[8], sa = 0.f, sb = 0.f;
#pragma unroll
        for (int i = 0; i < 4; ++i) { va[2 * i] = bflo(xa[i]); va[2 * i + 1] = bfhi(xa[i]); vb[2 * i] = bflo(xb[i]); vb[2 * i + 1] = bfhi(xb[i]); }
#pragma unroll
        for (int i = 0; i < 8; ++i) { sa += va[i]; sb += vb[i]; }
        sa = wave_sum(sa) * (1.f / 512.f); sb = wave_sum(sb) * (1.f / 512.f);
        float qa = 0.f, qb = 0.f;
#pragma unroll
        for (int i = 0; i < 8; ++i) { va[i] -= sa; vb[i] -= sb; qa = fmaf(va[i], va[i], qa); qb = fmaf(vb[i], vb[i], qb); }
        qa = __builtin_amdgcn_rsqf(wave_sum(qa) * (1.f / 512.f) + 1e-5f); qb = __builtin_amdgcn_rsqf(wave_sum(qb) * (1.f / 512.f) + 1e-5f);
        const u32x4 na = {pk2(va[0] * qa, va[1] * qa), pk2(va[2] * qa, va[3] * qa), pk2(va[4] * qa, va[5] * qa), pk2(va[6] * qa, va[7] * qa)};
        const u32x4 nb = {pk2(vb[0] * qb, vb[1] * qb), pk2(vb[2] * qb, vb[3] * qb), pk2(vb[4] * qb, vb[5] * qb), pk2(vb[6] * qb, vb[7] * qb)};
        *(LAS u32x4*)(L_LN + (2 * w) * 1088 + lane * 16) = na;
        *(LAS u32x4*)(L_LN + (2 * w + 1) * 1088 + lane * 16) = nb;
      }
      CFENCE();
      if (lane == 0) lds_inc(SY_LN);
      lds_wait_ge(SY_LN, 4);
      {
        const unsigned la = L_LN + (r16 >> 2) * 1088 + (128 * w + 16 * q + 4 * (lane & 3)) * 2;
#pragma unroll
        for (int i = 0; i < 2; ++i) {
          const bf16x8 f = lds_tr2(la + i * 128, la + i * 128 + 4 * 1088);
          __builtin_amdgcn_raw_buffer_store_b128(__builtin_bit_cast(u32x4, f), rws, h * 1024 + r32 * 16,
                                                 WS_NT + (unsigned)(((2 * w + i) * 32 + (b >> 3)) * 8 + ((b >> 1) & 3) * 2) * 1024u + (b & 1) * 512, 16);
        }
      }
      STAMP(14);
      asm volatile("s_waitcnt vmcnt(0)" : "+v"(sv), "+v"(ev)::"memory");
      basePQ = __builtin_amdgcn_readlane(sv, 0); baseG = __builtin_amdgcn_readlane(sv, 1);
      epoch = RFL(ev) + 1u;
      unsigned old = 0;
      if (lane == 0) old = lds_inc(SY_ND);
      if (RFL(old) == 3 && lane < 16) flag_st(ws + WS_NFLAG + lane * 1024 + (((b >> 1) & 3) * 64 + (b >> 3) * 2 + (b & 1)) * 4, epoch);
      STAMP(1);
      if (w == 0) { if (lane >= 2 && lane < 6) lds_set(SY_BAP + lane - 2, sv); if (lane == 6) lds_set(SY_EP, epoch); }
      {
        const char* fp = ws + WS_NFLAG + ((b >> 3) & 15) * 1024 + (w * 64 + lane) * 4;
_Pragma("unroll 1") for (int sp = 0; sp < SPIN_CAP; ++sp)
          if (all_lanes(atomic_peek(fp) == epoch)) break;
      }
#pragma unroll
      for (int s = 0; s < 4; ++s) dma2c(sws, lane * 16, WS_NT + (unsigned)(C_DN(b) * 32 + C_J0(b) + s) * 8192u + w * 2048, s * L_STAGE + 16384 + w * 2048);
      STAMP(2);
    }

    {
      f32x16 acc0 = {}, acc1 = {};
      f32x4 rsum = {};
      bf16x8 XN[8], XW[4], YN[8], YW[4];
      const unsigned vl = lane0 * 16;
      const unsigned wa = (32 * w + (lane0 & 31)) * 128 + (((lane0 >> 5) ^ ((lane0 >> 1) & 7)) * 16);
      const unsigned o1 = ((lane0 & 15) == ((lane0 >> 4) & 1)) ? 0x3f803f80u : 0u;
      const u32x4 ones4 = {o1, o1, o1, o1};
      const bf16x8 sel = __builtin_bit_cast(bf16x8, ones4);
      auto rd = [&](bf16x8 (&N)[8], bf16x8 (&Wf)[4], int s) {
        const unsigned sl = (unsigned)(s & 3) * L_STAGE;
        const unsigned nb = opv(vl + sl);
#pragma unroll
        for (int k = 0; k < 4; ++k) {
          Wf[k] = lds_frag((wa ^ (unsigned)(k * 32)) + sl);
          N[2 * k] = lds_frag(nb + 16384 + k * 2048);
          N[2 * k + 1] = lds_frag(nb + 16384 + k * 2048 + 1024);
        }
      };
      auto mm = [&](const bf16x8 (&N)[8], const bf16x8 (&Wf)[4]) {
#pragma unroll
        for (int k = 0; k < 4; ++k) {
          acc0 = MFMA32(N[2 * k], Wf[k], acc0);
          acc1 = MFMA32(N[2 * k + 1], Wf[k], acc1);
          rsum = MFMA16(sel, Wf[k], rsum);
        }
      };
      auto pin = [&]() {
        SGB(0x8, 1); SGB(0x2, 5);
#pragma unroll
        for (int i = 0; i < 6; ++i) { SGB(0x100, 2); SGB(0x8, 1); }
        SGB(0x8, 5);
      };
      WAITV(6); BAR();
      STAMP(3);
      rd(XN, XW, 0);
      WAITL0();
      WAITV(4);
#pragma unroll 1
      for (int s = 1; s < 31; s += 2) {
        SCHED(); BAR(); SCHED();
        rd(YN, YW, s);
        mm(XN, XW);
        pin();
        WAITL0();
        SCHED();
        if (s == 1) {
          WAITV(2);
        } else if (s == 3) {
          const int b = ops(b0);
          if (b == 0 && w == 0 && lane0 == 0) flag_st(ws + WS_NONCE, epoch);
          dma1w(mk_srd(p.c, 1024u), opv(lane0) * 4, (b & 3) * 256, L_HK + w * 2304);
        } else if (s == 5) {
          const int b = ops(b0);
          const unsigned lane = opv(lane0), q = lane >> 4, r16 = lane & 15;
          WAITV(0);
          LAS unsigned* cp = (LAS unsigned*)(L_HK + w * 2304 + lane * 4);
          const unsigned cc = *cp;
          const float c0 = bflo(cc), c1 = bfhi(cc);
          *cp = pk2(c0 * __builtin_amdgcn_rcpf(1.f + __builtin_amdgcn_exp2f(c0 * -1.44269504f)), c1 * __builtin_amdgcn_rcpf(1.f + __builtin_amdgcn_exp2f(c1 * -1.44269504f)));
          f32x4 d = {};
          const unsigned ma = L_AUX + (8 * q + (r16 >> 2)) * 128 + (16 * w + 4 * (lane & 3)) * 2;
#pragma unroll
          for (int k = 0; k < 4; ++k) d = MFMA16(lds_frag(L_HK + w * 2304 + (32 * k + 8 * q) * 2), lds_tr2(ma + k * 4096, ma + k * 4096 + 512), d);
          if (b < 192 && lane < 16) __builtin_amdgcn_raw_buffer_store_b32(f2u(d[0]), rws, lane * 4, WS_MODP + (unsigned)((b & 3) * 3072 + (b >> 2) * 64 + 16 * w) * 4u, 16);
          WAITL0();
        } else if (s == 7) {
          const int b = ops(b0);
          const unsigned lane = opv(lane0);
          WAITV(0);
          if (b < 224) {
            const int akb = b / 28;
            const unsigned g0 = (unsigned)(64 * akb + 16 * w + (lane >> 3)) * 3584u + (lane & 7) * 16, g1 = g0 + 8 * 3584 - 1024;
            dma2v(mk_srd(p.wg, 512u * 1792u * 2u), g0, g1, (b % 28) * 128, L_AUX + w * 2048);
            dma2v(mk_srd(p.wu, 512u * 1792u * 2u), g0, g1, (b % 28) * 128, L_AUX + 8192 + w * 2048);
            const unsigned d0 = (unsigned)(64 * (b >> 3) + 16 * w + (lane >> 3)) * 1024u + (lane & 7) * 16, d1 = d0 + 8 * 1024 - 1024;
            dma2v(mk_srd(p.wdn, 1792u * 512u * 2u), d0, d1, (b & 7) * 128, L_AUX + 16384 + w * 2048);
          }
        } else if (s == 9) {
          const int b = ops(b0);
          if (b < 192 && w == 0 && lane0 == 0) flag_st(ws + WS_MODFLAG + b * 4, epoch);
        } else if (s == 13) {
          const int b = ops(b0);
          const unsigned lane = opv(lane0);
          const unsigned v = (lane >> 2) > 6 ? 6 : lane >> 2;
          const unsigned cbk = (unsigned)((0x18202820100800ull >> (8 * v)) & 0xff) + (v < 5 ? C_DN(b) : (b / 28) & 7);
          dma1w(sws, cbk * 16 + (lane & 3) * 4, WS_MODFLAG, L_HK + w * 2304);
        } else if (s == 15) {
          const int b = ops(b0);
          const unsigned lane = opv(lane0);
          const int akb = (b / 28) & 7, dn = C_DN(b);
          WAITV(0);
          if (!all_lanes(*(volatile LAS unsigned*)(L_HK + w * 2304 + lane * 4) == epoch)) {
            const unsigned v = (lane >> 2) > 6 ? 6 : lane >> 2;
            const unsigned cbk = (unsigned)((0x18202820100800ull >> (8 * v)) & 0xff) + (v < 5 ? dn : akb);
_Pragma("unroll 1") for (int sp = 0; sp < SPIN_CAP; ++sp)
              if (all_lanes(flag_ld(ws + WS_MODFLAG + cbk * 16 + (lane & 3) * 4) == epoch)) break;
          }
          CFENCE();
          const int cb0 = (w == 3 ? 32 : 8 * w) + dn;
          const int cb1 = w == 0 ? 40 + dn : w == 1 ? 32 + akb : 24 + akb;
#pragma unroll
          for (int k = 0; k < 4; ++k) {
            dma1w(sws, lane * 4, WS_MODP + (unsigned)(k * 3072 + cb0 * 64) * 4u, L_HK + w * 2304 + 256 + k * 256);
            dma1w(sws, lane * 4, WS_MODP + (unsigned)(k * 3072 + cb1 * 64) * 4u, L_HK + w * 2304 + 1280 + k * 256);
          }
        } else if (s == 17) {
          const unsigned lane = opv(lane0);
          WAITV(0);
          const LAS float* mp = (const LAS float*)(L_HK + w * 2304 + 256 + lane * 4);
          float m0 = rbf((mp[0] + mp[64]) + (mp[128] + mp[192])), m1 = rbf((mp[256] + mp[320]) + (mp[384] + mp[448]));
          if (w & 1) m0 = rbf(1.f + m0);
          if (w == 1) m1 = rbf(1.f + m1);
          if (w < 2 && C_PART(ops(b0))) m0 = -m0;
          *(LAS float*)(L_MOD + w * 256 + lane * 4) = m0;
          if (w == 0) *(LAS float*)(L_MOD + 1024 + lane * 4) = m1;
          else if (w < 3) *(LAS u16*)(L_VB + (w - 1) * 128 + lane * 2) = (u16)(f2u(m1) >> 16);
          WAITL0();
        } else if (s == 19) {
          const int b = ops(b0);
          if (b < 224) {
            const unsigned lane = opv(lane0), h = lane >> 5, q = lane >> 4, r16 = lane & 15;
            const int akb = b / 28, acb = b % 28;
            f32x16 d = {};
            const unsigned ga = L_AUX + (q & 1) * 8192 + (8 * h + (r16 >> 2)) * 128 + (16 * w + 4 * (lane & 3)) * 2;
#pragma unroll
            for (int k = 0; k < 4; ++k) {
              const bf16x8 f = lds_tr2(ga + k * 2048, ga + k * 2048 + 512);
              __builtin_amdgcn_raw_buffer_store_b128(__builtin_bit_cast(u32x4, f), rws, lane * 16 + k * 1024, WS_WT + (unsigned)((4 * acb + w) * 32 + 4 * akb) * 1024u, 16);
              d = MFMA32(f, lds_frag(L_VB + (lane & 1) * 128 + (16 * k + 8 * h) * 2), d);
            }
            const unsigned da = L_AUX + 16384 + (16 * (q >> 1) + 4 * (q & 1) + (r16 >> 2)) * 128 + (16 * w + 4 * (lane & 3)) * 2;
#pragma unroll
            for (int k = 0; k < 2; ++k) {
              const bf16x8 f = lds_tr2(da + k * 4096, da + k * 4096 + 1024);
              __builtin_amdgcn_raw_buffer_store_b128(__builtin_bit_cast(u32x4, f), rws, lane * 16, WS_WDT + (unsigned)(((b & 7) * 28 + (b >> 3)) * 8 + 4 * k + w) * 1024u, 16);
            }
            if ((lane & 31) < 2) {
#pragma unroll
              for (int g = 0; g < 4; ++g) {
                const u32x4 o = {f2u(d[4 * g]), f2u(d[4 * g + 1]), f2u(d[4 * g + 2]), f2u(d[4 * g + 3])};
                __builtin_amdgcn_raw_buffer_store_b128(o, rws, (h * 2 + (lane & 31)) * 64 + g * 16, WS_UVP + (unsigned)(akb * 112 + 4 * acb + w) * 256u, 16);
              }
            }
            WAITL0();
          }
        } else if (s == 21) {
          const int b = ops(b0);
          WAITV(0);
          const unsigned blk = 4 * (C_TB64(b) & 3) + w;
          dma1w(mk_srd(blk >= 8 ? p.wdim : p.wdre, 512u * 512u * 2u), opv(lane0) * 1024, (unsigned)C_DN(b) * 65536u + (blk & 7) * 128, L_JUNK);
        } else if (s == 23) {
          const int b = ops(b0);
          if (b < 224 && w == 0 && lane0 == 0) flag_st(ws + WS_AUXFLAG + b * 4, epoch);
        }
        SCHED(); BAR(); SCHED();
        rd(XN, XW, s + 1);
        mm(YN, YW);
        pin();
        WAITL0();
        if (s == 1) WAITV(0);
      }
      SCHED(); BAR(); SCHED();
      rd(YN, YW, 31);
      mm(XN, XW);
      pin();
      SCHED();
      const unsigned lane = opv(lane0), h = lane >> 5;
      const unsigned tb = opv(L_MOD + h * 16);
      f32x4 sa[8], c1[8];
#pragma unroll
      for (int g = 0; g < 8; ++g) { sa[g] = lds_f4(tb + g * 32); c1[g] = lds_f4(tb + 256 + g * 32); }
      SCHED();
#pragma unroll
      for (int k = 0; k < 4; ++k) rsum = MFMA16(sel, YW[k], rsum);
#pragma unroll
      for (int k = 0; k < 4; ++k) { acc0 = MFMA32(YN[2 * k], YW[k], acc0); }
      const float r0 = u2f(__builtin_amdgcn_ds_bpermute((lane & 15) * 4, f2u(rsum[0]))), r1 = u2f(__builtin_amdgcn_ds_bpermute((lane & 15) * 4, f2u(rsum[1])));
#pragma unroll
      for (int k = 0; k < 4; ++k) { acc1 = MFMA32(YN[2 * k + 1], YW[k], acc1); }
      SCHED();
      STAMP(4);
      const int b = ops(b0);
      const float rs = (lane & 16) ? r1 : r0;
      const unsigned vo = ((lane >> 4) & 1) * 32768 + h * 256 + (lane & 15) * 16;
      const unsigned so = WS_PQ + (unsigned)((8 * C_TP(b) + 2 * w) * 32 + C_PART(b) * 16 + 2 * C_DN(b)) * 1024u;
#pragma unroll
      for (int db = 0; db < 2; ++db) {
        unsigned pk[8];
#pragma unroll
        for (int g = 0; g < 4; ++g) {
          float v[4];
#pragma unroll
          for (int j = 0; j < 4; ++j) v[j] = fmaf(db ? acc1[4 * g + j] : acc0[4 * g + j], c1[4 * db + g][j], rs * sa[4 * db + g][j]);
          pk[2 * g] = pk2(v[0], v[1]); pk[2 * g + 1] = pk2(v[2], v[3]);
        }
#pragma unroll
        for (int hf = 0; hf < 2; ++hf) {
          auto s0 = __builtin_amdgcn_permlane32_swap(pk[4 * hf], pk[4 * hf + 2], false, false);
          auto s1 = __builtin_amdgcn_permlane32_swap(pk[4 * hf + 1], pk[4 * hf + 3], false, false);
          const u32x4 o = {s0[0], s1[0], s0[1], s1[1]};
          __builtin_amdgcn_raw_buffer_store_b128(o, rws, vo + db * 1024 + hf * 512, so, 16);
        }
      }
      WAITV(0);
      if (lane == 0) cnt_add(ws + WS_CPQ + (4 * C_TP(b) + w) * 256);
      STAMP(5);
    }

    float x1[16];
    {
      const int b = ops(b0);
      const unsigned lane = opv(lane0), q = lane >> 4, r16 = lane & 15, vl = lane * 16;
      const int tb64 = C_TB64(b), et = C_DN(b), tb16 = 4 * tb64 + w, t32c = 2 * tb64 + (w >> 1);
      u32x2 xr[4];
      {
        const __amdgpu_buffer_rsrc_t rx = mk_rsrc(p.x, 2048u * 512u * 2u);
#pragma unroll
        for (int e = 0; e < 4; ++e) xr[e] = __builtin_amdgcn_raw_buffer_load_b64(rx, r16 * 1024 + q * 8 + e * 32, (unsigned)(16 * tb16) * 1024u + et * 128, 0);
      }
      cnt_wait(ws + WS_CPQ + t32c * 256, basePQ, lane, 1);
      STAMP(6);
      bf16x8 pq[32];
#pragma unroll
      for (int n = 0; n < 32; ++n)
        pq[n] = __builtin_bit_cast(bf16x8, __builtin_amdgcn_raw_buffer_load_b128(rws, vl + (n & 3) * 1024, WS_PQ + (unsigned)(tb16 * 32 + (((n >> 2) + et) & 7) * 4) * 1024u, 16));
      SCHED();
      lds_wait4(SY_WD);
      SCHED();
      f32x4 y[4] = {};
      const unsigned la = r16 * 128 + ((q ^ (r16 >> 1)) * 16);
      bf16x8 F[2][8];
      auto rdw = [&](bf16x8 (&f)[8], int m) {
        int nl = ((m + 2 * et - 4 * (tb64 & 3)) & 15) + 12;
        nl = nl >= 19 ? nl - 19 : nl;
        const unsigned a0 = opv(la + (unsigned)nl * 8192u), a1 = opv((la ^ 64u) + (unsigned)nl * 8192u);
#pragma unroll
        for (int e = 0; e < 4; ++e) f[e] = lds_frag(a0 + e * 2048);
#pragma unroll
        for (int e = 0; e < 4; ++e) f[4 + e] = lds_frag(a1 + e * 2048);
      };
      rdw(F[0], 0);
#pragma unroll
      for (int m = 0; m < 16; ++m) {
        SCHED();
        if (m < 15) rdw(F[(m + 1) & 1], m + 1);
#pragma unroll
        for (int i = 0; i < 8; ++i) y[i & 3] = MFMA16(F[m & 1][i], pq[2 * m + (i >> 2)], y[i & 3]);
        if (m < 15) {
#pragma unroll
          for (int i = 0; i < 8; ++i) { SGB(0x100, 1); SGB(0x8, 1); }
        }
      }
      SCHED();
      STAMP(7);
      const unsigned tb = opv(L_MOD + 512 + q * 16);
      f32x4 ga[4], c1[4];
#pragma unroll
      for (int e = 0; e < 4; ++e) { ga[e] = lds_f4(tb + e * 64); c1[e] = lds_f4(tb + 256 + e * 64); }
      unsigned pk[8];
      float sm = 0.f;
#pragma unroll
      for (int e = 0; e < 4; ++e) {
        x1[4 * e] = fmaf(ga[e][0], y[e][0], bflo(xr[e][0])); x1[4 * e + 1] = fmaf(ga[e][1], y[e][1], bfhi(xr[e][0]));
        x1[4 * e + 2] = fmaf(ga[e][2], y[e][2], bflo(xr[e][1])); x1[4 * e + 3] = fmaf(ga[e][3], y[e][3], bfhi(xr[e][1]));
        pk[2 * e] = pk2(x1[4 * e] * c1[e][0], x1[4 * e + 1] * c1[e][1]); pk[2 * e + 1] = pk2(x1[4 * e + 2] * c1[e][2], x1[4 * e + 3] * c1[e][3]);
        sm += (x1[4 * e] + x1[4 * e + 1]) + (x1[4 * e + 2] + x1[4 * e + 3]);
      }
      const unsigned vo = (q & 1) * 1024 + (q >> 1) * 512 + (w & 1) * 256 + r16 * 16;
#pragma unroll
      for (int pr = 0; pr < 2; ++pr) {
        auto s0 = __builtin_amdgcn_permlane16_swap(pk[4 * pr], pk[4 * pr + 2], false, false);
        auto s1 = __builtin_amdgcn_permlane16_swap(pk[4 * pr + 1], pk[4 * pr + 3], false, false);
        const u32x4 o = {s0[0], s1[0], s0[1], s1[1]};
        __builtin_amdgcn_raw_buffer_store_b128(o, rws, vo + pr * 2048, WS_AP + (unsigned)(t32c * 32 + 4 * et) * 1024u, 16);
      }
      const float mu = sum32(sum16(sm)) * (1.f / 64.f);
      float m2 = 0.f;
#pragma unroll
      for (int i = 0; i < 16; ++i) { const float d = x1[i] - mu; m2 = fmaf(d, d, m2); }
      m2 = sum32(sum16(m2));
      if (lane < 16) {
        const u32x2 o = {f2u(mu), f2u(m2)};
        __builtin_amdgcn_raw_buffer_store_b64(o, rws, lane * 8, WS_STATS + (unsigned)(et * 2048 + 16 * tb16) * 8u, 16);
      }
      WAITL0();
      BAR();
      WAITV(0);
      if (lane == 0) cnt_add(ws + WS_CAP + t32c * 256);
      STAMP(8);
    }
    {
      const int b = ops(b0);
      const unsigned lane = opv(lane0);
      bf16x8 W[32];
      u32x4 uv[2];
      load_w(W, uv, rws, 7 * C_FT(b) + w, C_LOC(b) >> 2, lane);
      k4_body(W, uv, rws, ws, w, lane, C_TP4(b), C_FT(b), C_LOC(b) >> 2);
    }
    STAMP(9);

    {
      const int b = ops(b0);
      const unsigned lane = opv(lane0), q = lane >> 4, r16 = lane & 15, vl = lane * 16;
      const int tb64 = C_TB64(b), et = C_DN(b), tb16 = 4 * tb64 + w, t32c = 2 * tb64 + (w >> 1);
      cnt_wait(ws + WS_CG + t32c * 256, baseG, lane, 1);
      STAMP(10);
      const int rot5 = C_ROT5(b);
      const unsigned gb0 = WS_G + (unsigned)tb16 * 57344u;
      bf16x8 g[14];
      auto ldg = [&](int n, bf16x8& lo, bf16x8& hi) {
        int kb = n + rot5; kb = kb >= 28 ? kb - 28 : kb;
        lo = __builtin_bit_cast(bf16x8, __builtin_amdgcn_raw_buffer_load_b128(rws, vl, gb0 + (unsigned)kb * 2048u, 16));
        hi = __builtin_bit_cast(bf16x8, __builtin_amdgcn_raw_buffer_load_b128(rws, vl + 1024, gb0 + (unsigned)kb * 2048u, 16));
      };
#pragma unroll
      for (int m = 0; m < 7; ++m) ldg(m, g[2 * m], g[2 * m + 1]);
      f32x4 y[4] = {};
      bf16x8 F[2][8];
      auto rdw = [&](bf16x8 (&f)[8], int n) {
        const unsigned a0 = opv(vl + (unsigned)((16 + n) % 19) * 8192u);
#pragma unroll
        for (int e = 0; e < 8; ++e) f[e] = lds_frag(a0 + e * 1024);
      };
      lds_wait_ge(SY_WL + 0, 1);
      rdw(F[0], 0);
#pragma unroll
      for (int n = 0; n < 28; ++n) {
        const int m = n % 7;
        SCHED();
        if (m == 6 && n < 27) lds_wait_ge(SY_WL + n / 7 + 1, 1);
        SCHED();
        if (n < 27) rdw(F[(n + 1) & 1], n + 1);
#pragma unroll
        for (int i = 0; i < 8; ++i) y[i & 3] = MFMA16(F[n & 1][i], g[2 * m + (i >> 2)], y[i & 3]);
        if (n < 27) {
#pragma unroll
          for (int i = 0; i < 8; ++i) { SGB(0x100, 1); SGB(0x8, 1); }
        }
        SCHED();
        if (n < 21) ldg(n + 7, g[2 * m], g[2 * m + 1]);
        if (n == 6 || n == 13) { if (lane == 0) lds_inc(SY_PROG + n / 7); }
        if (m == 6) STAMP(24 + n / 7);
      }
      SCHED();
      STAMP(11);
      const unsigned tb = opv(L_MOD + 1024 + q * 16);
      unsigned pk[8];
#pragma unroll
      for (int e = 0; e < 4; ++e) {
        const f32x4 gb = lds_f4(tb + e * 64);
        pk[2 * e] = pk2(fmaf(gb[0], y[e][0], x1[4 * e]), fmaf(gb[1], y[e][1], x1[4 * e + 1]));
        pk[2 * e + 1] = pk2(fmaf(gb[2], y[e][2], x1[4 * e + 2]), fmaf(gb[3], y[e][3], x1[4 * e + 3]));
      }
      const __amdgpu_buffer_rsrc_t ro = mk_rsrc(p.out, 2048u * 512u * 2u);
#pragma unroll
      for (int pr = 0; pr < 2; ++pr) {
        auto s0 = __builtin_amdgcn_permlane16_swap(pk[4 * pr], pk[4 * pr + 2], false, false);
        auto s1 = __builtin_amdgcn_permlane16_swap(pk[4 * pr + 1], pk[4 * pr + 3], false, false);
        const u32x4 o = {s0[0], s1[0], s0[1], s1[1]};
        __builtin_amdgcn_raw_buffer_store_b128(o, ro, r16 * 1024 + (q & 1) * 32 + (q >> 1) * 16 + pr * 64, (unsigned)(16 * tb16) * 1024u + et * 128, 0);
      }
    }
    STAMP(12);
  }
}

#if defined(__HIP_DEVICE_COMPILE__)
#pragma clang attribute pop
#endif

extern "C" void kernel_launch(void* const* d_in, const int* in_sizes, int n_in, void* d_out, int out_size, void* d_ws, size_t ws_size,
                              hipStream_t stream) {
  Params p;
  p.x = (const u16*)d_in[0]; p.wsre = (const u16*)d_in[1]; p.wsim = (const u16*)d_in[2];
  p.wdre = (const u16*)d_in[3]; p.wdim = (const u16*)d_in[4];
  p.wg = (const u16*)d_in[6]; p.wu = (const u16*)d_in[7]; p.wdn = (const u16*)d_in[8];
  p.c = (const u16*)d_in[9]; p.wmod = (const u16*)d_in[10];
  p.out = (u16*)d_out; p.ws = (char*)d_ws;
  (void)hipFuncSetAttribute((const void*)fused_block, hipFuncAttributeMaxDynamicSharedMemorySize, LDS_BYTES);
  fused_block<<<256, 512, LDS_BYTES, stream>>>(p);
}
```

```cpp
#include <hip/hip_runtime.h>

typedef unsigned short u16;
typedef unsigned long long u64;
typedef short bf16x8 __attribute__((ext_vector_type(8)));
typedef short s16x4 __attribute__((ext_vector_type(4)));
typedef __bf16 bf2 __attribute__((ext_vector_type(2)));
typedef float f32x16 __attribute__((ext_vector_type(16)));
typedef float f32x4 __attribute__((ext_vector_type(4)));
typedef float f32x2 __attribute__((ext_vector_type(2)));
typedef unsigned u32x4 __attribute__((ext_vector_type(4)));
typedef unsigned u32x2 __attribute__((ext_vector_type(2)));
#define DI __device__ __forceinline__
#define LAS __attribute__((address_space(3)))
#define GAS __attribute__((address_space(1)))

constexpr unsigned WS_NONCE = 0;
constexpr unsigned WS_NFLAG = 98304 + 16384;
constexpr unsigned WS_MODFLAG = 4096;
constexpr unsigned WS_AUXFLAG = 8192;
constexpr unsigned WS_CPQ = 16384;
constexpr unsigned WS_CAP = 32768;
constexpr unsigned WS_CG = 49152;
constexpr unsigned WS_MODP = 65536;
constexpr unsigned WS_UVP = 131072;
constexpr unsigned WS_STATS = 393216;
constexpr unsigned WS_RSP = 524288;
constexpr unsigned WS_NT = 1u << 20;
constexpr unsigned WS_PQ = 4u << 20;
constexpr unsigned WS_AP = 8u << 20;
constexpr unsigned WS_G = 10u << 20;
constexpr unsigned WS_WT = 18u << 20;
constexpr unsigned WS_WDT = 22u << 20;
constexpr unsigned WS_DIAG = 24u << 20;
constexpr unsigned WS_BYTES = 26u << 20;

constexpr unsigned L_STAGE = 24576;
constexpr unsigned L_AUX = 98304;
constexpr unsigned L_LN = 122880;
constexpr unsigned L_HK = 132096;
constexpr unsigned L_TAB = 155648;
constexpr unsigned L_MOD = L_TAB;
constexpr unsigned L_VB = L_TAB + 1280;
constexpr unsigned L_UV = L_TAB + 1536;
constexpr unsigned L_ST = L_TAB + 3328;
constexpr unsigned L_SY = L_TAB + 4352;
constexpr unsigned LDS_BYTES = 163840;
enum { SY_ADONE = 0, SY_STAT = 4, SY_UFIN = 5, SY_GSIG = 9, SY_WL = 10, SY_PROG = 14, SY_LN = 16, SY_ND = 17, SY_AUX = 18, SY_WD = 20, SY_X3 = 24, SY_X5 = 28, SY_N = 32, SY_EP = 32, SY_BAP = 36 };
constexpr unsigned L_RS = L_TAB + 6400;
constexpr unsigned HK = 3328;
#define AUXW 23
#define AUXL 9
#define L_PACE 6
#define G_WIN 4
#define L_EARLY 22
#define W_PACE 4
#define WD_PACE 4
#define A_PACE 4
#define W_EARLY 5

constexpr int SPIN_CAP = 1 << 20;
constexpr int CODE_LINES_PER_WG = 10;

struct Params {
  const u16 *x, *wsre, *wsim, *wdre, *wdim, *wg, *wu, *wdn, *c, *wmod;
  u16* out;
  char* ws;
};

#if defined(__HIP_DEVICE_COMPILE__)
#pragma clang attribute push(__attribute__((target("no-packed-fp32-ops"))), apply_to = function)
#endif
DI float u2f(unsigned u) { return __builtin_bit_cast(float, u); }
DI unsigned f2u(float f) { return __builtin_bit_cast(unsigned, f); }
DI bool all_lanes(bool c) { return __builtin_amdgcn_ballot_w64(c) == ~0ull; }
DI u32x4 mk_srd(const void* p, unsigned bytes) {
  u64 a = (u64)p;
  u32x4 r = {(unsigned)a, (unsigned)(a >> 32) & 0xffffu, bytes, 0x00020000u};
  return r;
}
DI __amdgpu_buffer_rsrc_t mk_rsrc(const void* p, unsigned bytes) {
  return __builtin_amdgcn_make_buffer_rsrc((void*)p, 0, bytes, 0x00020000);
}
#define RFL(x) __builtin_amdgcn_readfirstlane(x)
#define BAR() __builtin_amdgcn_s_barrier()
#define WAITV(n) asm volatile("s_waitcnt vmcnt(%0)" ::"i"(n) : "memory")
#define WAITL0() __builtin_amdgcn_s_waitcnt(0xC07F)
#define CFENCE() asm volatile("" ::: "memory")

DI void dma4v(u32x4 srd, unsigned v0, unsigned v1, unsigned v2, unsigned v3, unsigned soff, unsigned m0v) {
  asm volatile("s_mov_b32 m0, %0\n\ts_nop 0\n\t"
               "buffer_load_dwordx4 %1, %5, %6 offen lds\n\t"
               "buffer_load_dwordx4 %2, %5, %6 offen offset:1024 lds\n\t"
               "buffer_load_dwordx4 %3, %5, %6 offen offset:2048 lds\n\t"
               "buffer_load_dwordx4 %4, %5, %6 offen offset:3072 lds"
               ::"s"(m0v), "v"(v0), "v"(v1), "v"(v2), "v"(v3), "s"(srd), "s"(soff) : "memory");
}
DI void dma2v(u32x4 srd, unsigned v0, unsigned v1, unsigned soff, unsigned m0v) {
  asm volatile("s_mov_b32 m0, %0\n\ts_nop 0\n\t"
               "buffer_load_dwordx4 %1, %3, %4 offen lds\n\t"
               "buffer_load_dwordx4 %2, %3, %4 offen offset:1024 lds"
               ::"s"(m0v), "v"(v0), "v"(v1), "s"(srd), "s"(soff) : "memory");
}
DI void dma2n(u32x4 srd, unsigned v0, unsigned v1, unsigned soff, unsigned m0v) {
  asm volatile("s_mov_b32 m0, %0\n\ts_nop 0\n\t"
               "buffer_load_dwordx4 %1, %3, %4 offen nt lds\n\t"
               "buffer_load_dwordx4 %2, %3, %4 offen offset:1024 nt lds"
               ::"s"(m0v), "v"(v0), "v"(v1), "s"(srd), "s"(soff) : "memory");
}
DI void dma2c(u32x4 srd, unsigned v, unsigned soff, unsigned m0v) {
  asm volatile("s_mov_b32 m0, %0\n\ts_nop 0\n\t"
               "buffer_load_dwordx4 %1, %2, %3 offen sc1 lds\n\t"
               "buffer_load_dwordx4 %1, %2, %3 offen offset:1024 sc1 lds"
               ::"s"(m0v), "v"(v), "s"(srd), "s"(soff) : "memory");
}
DI void dma4c(u32x4 srd, unsigned v, unsigned soff, unsigned m0v) {
  asm volatile("s_mov_b32 m0, %0\n\ts_nop 0\n\t"
               "buffer_load_dwordx4 %1, %2, %3 offen sc1 lds\n\t"
               "buffer_load_dwordx4 %1, %2, %3 offen offset:1024 sc1 lds\n\t"
               "buffer_load_dwordx4 %1, %2, %3 offen offset:2048 sc1 lds\n\t"
               "buffer_load_dwordx4 %1, %2, %3 offen offset:3072 sc1 lds"
               ::"s"(m0v), "v"(v), "s"(srd), "s"(soff) : "memory");
}
DI void dma1w(u32x4 srd, unsigned v, unsigned soff, unsigned m0v) {
  asm volatile("s_mov_b32 m0, %0\n\ts_nop 0\n\tbuffer_load_dword %1, %2, %3 offen sc1 lds" ::"s"(m0v), "v"(v), "s"(srd), "s"(soff) : "memory");
}
DI unsigned opv(unsigned v) { asm volatile("" : "+v"(v)); return v; }
DI int ops(int v) { asm volatile("" : "+s"(v)); return v; }
#define SCHED() __builtin_amdgcn_sched_barrier(0)
#define SGB(m, n) __builtin_amdgcn_sched_group_barrier((m), (n), 0)
DI bf16x8 lds_frag(unsigned a) { return *(const LAS bf16x8*)a; }
DI f32x4 lds_f4(unsigned a) { return *(const LAS f32x4*)a; }
DI bf16x8 lds_tr2(unsigned a0, unsigned a1) {
  s16x4 lo = __builtin_amdgcn_ds_read_tr16_b64_v4i16((LAS s16x4*)a0);
  s16x4 hi = __builtin_amdgcn_ds_read_tr16_b64_v4i16((LAS s16x4*)a1);
  return __builtin_shufflevector(lo, hi, 0, 1, 2, 3, 4, 5, 6, 7);
}
DI unsigned pk2(float a, float b) { bf2 p = {(__bf16)a, (__bf16)b}; return __builtin_bit_cast(unsigned, p); }
DI float bflo(unsigned u) { return u2f(u << 16); }
DI float bfhi(unsigned u) { return u2f(u & 0xffff0000u); }
DI float rbf(float a) { return (float)(__bf16)a; }
DI float sum32(float v) { auto r = __builtin_amdgcn_permlane32_swap(f2u(v), f2u(v), false, false); return u2f(r[0]) + u2f(r[1]); }
DI float sum16(float v) { auto r = __builtin_amdgcn_permlane16_swap(f2u(v), f2u(v), false, false); return u2f(r[0]) + u2f(r[1]); }
template <int CTRL> DI float dpp_add(float v) { return v + u2f(__builtin_amdgcn_update_dpp(0u, f2u(v), CTRL, 0xf, 0xf, true)); }
DI float wave_sum(float v) {
  v = dpp_add<0xB1>(v);
  v = dpp_add<0x4E>(v);
  v = dpp_add<0x141>(v);
  v = dpp_add<0x140>(v);
  return sum32(sum16(v));
}
#define MFMA16(a, b, c) __builtin_amdgcn_mfma_f32_16x16x32_bf16((a), (b), (c), 0, 0, 0)

DI unsigned atomic_peek(const char* p) {
  unsigned r, z = 0;
  asm volatile("global_atomic_add %0, %1, %2, off sc0\n\ts_waitcnt vmcnt(0)" : "=&v"(r) : "v"(p), "v"(z) : "memory");
  return r;
}
DI void cnt_add(char* p) { asm volatile("global_atomic_add %0, %1, off" ::"v"(p), "v"(1u) : "memory"); }
DI unsigned flag_ld(const char* p) { return __hip_atomic_load((GAS unsigned*)p, __ATOMIC_RELAXED, __HIP_MEMORY_SCOPE_AGENT); }
DI void flag_st(char* p, unsigned v) { asm volatile("global_atomic_swap %0, %1, off" ::"v"(p), "v"(v) : "memory"); }
DI void cnt_wait(const char* p, unsigned base, int lane, int nl) {
_Pragma("unroll 1") for (int sp = 0; sp < SPIN_CAP; ++sp) {
    unsigned v = base + 16u;
    if (lane < nl) v = atomic_peek(p);
    if (all_lanes(v - base >= 16u)) break;
  }
  CFENCE();
}
DI void lds_wait_ge(unsigned word, unsigned want) {
  volatile LAS unsigned* p = (volatile LAS unsigned*)(L_SY + word * 4);
_Pragma("unroll 1") for (int sp = 0; sp < SPIN_CAP; ++sp) {
    if (RFL(*p) >= want) break;
    __builtin_amdgcn_s_sleep(1);
  }
  CFENCE();
}
DI void lds_wait4(unsigned word) {
  volatile LAS u32x4* p = (volatile LAS u32x4*)(L_SY + word * 4);
_Pragma("unroll 1") for (int sp = 0; sp < SPIN_CAP; ++sp) {
    const u32x4 v = *p;
    if (RFL(v[0] & v[1] & v[2] & v[3])) break;
    __builtin_amdgcn_s_sleep(1);
  }
  CFENCE();
}
DI unsigned lds_get(unsigned word) { return *(volatile LAS unsigned*)(L_SY + word * 4); }
DI void lds_set(unsigned word, unsigned v) { *(volatile LAS unsigned*)(L_SY + word * 4) = v; }
DI unsigned lds_inc(unsigned word) { return __hip_atomic_fetch_add((LAS unsigned*)(L_SY + word * 4), 1u, __ATOMIC_RELAXED, __HIP_MEMORY_SCOPE_WORKGROUP); }

#define STAMP(i)
#define STAMPB(i)

template <int I> struct IC { static constexpr int value = I; };
template <int A, int B, class F> DI void static_for(F&& f) { if constexpr (A < B) { f(IC<A>{}); static_for<A + 1, B>(f); } }

DI u32x4 merge16(unsigned a0, unsigned a1, unsigned b0, unsigned b1) {
  auto s0 = __builtin_amdgcn_permlane16_swap(a0, b0, false, false);
  auto s1 = __builtin_amdgcn_permlane16_swap(a1, b1, false, false);
  const u32x4 o = {s0[0], s1[0], s0[1], s1[1]};
  return o;
}

DI void pair_reduce(f32x4 (&y)[4], const f32x4 (&y2)[2][4], unsigned scratch, unsigned word, int w, unsigned lane) {
  const bool odd = w & 1;
  const unsigned a = scratch + lane * 16;
#pragma unroll
  for (int e = 0; e < 4; ++e) *(LAS f32x4*)(a + w * 4096 + e * 1024) = odd ? y2[0][e] : y2[1][e];
  CFENCE();
  lds_set(word + w, 1);
  lds_wait_ge(word + (w ^ 1), 1);
#pragma unroll
  for (int e = 0; e < 4; ++e) y[e] = (odd ? y2[1][e] : y2[0][e]) + lds_f4(a + (w ^ 1) * 4096 + e * 1024);
}

DI void load_w(bf16x8 (&W)[32], u32x4 (&uv)[2], __amdgpu_buffer_rsrc_t rws, int c, int rot, unsigned lane) {
  const unsigned uo = (lane >> 4) * 28672 + (lane & 15) * 16;
  uv[0] = __builtin_amdgcn_raw_buffer_load_b128(rws, uo, WS_UVP + (unsigned)c * 256u, 16);
  uv[1] = __builtin_amdgcn_raw_buffer_load_b128(rws, uo, WS_UVP + (unsigned)(448 + c) * 256u, 16);
  const unsigned vl = opv(lane * 16);
#pragma unroll
  for (int k = 0; k < 32; ++k) {
    W[k] = __builtin_bit_cast(bf16x8, __builtin_amdgcn_raw_buffer_load_b128(rws, vl + (k & 3) * 1024, WS_WT + (unsigned)(c * 32 + (((k >> 2) + rot) & 7) * 4) * 1024u, 16));
    if ((k & 1) == 1 && k >= W_PACE && k < 31) WAITV(W_PACE);
  }
  SCHED();
}

DI float geglu(float g, float u, float rstd, float nmr, float ug, float vg, float uu, float vu) {
  const float a = fmaf(g, rstd, fmaf(nmr, ug, vg));
  const float b = fmaf(u, rstd, fmaf(nmr, uu, vu));
  const float e = a * fmaf(a * a, -0.10294324f, -2.30220820f);
  return a * b * __builtin_amdgcn_rcpf(1.f + __builtin_amdgcn_exp2f(e));
}

DI void k4_body(const bf16x8 (&W)[32], const u32x4 (&uv)[2], __amdgpu_buffer_rsrc_t rws, char* ws, int wv, unsigned lane, int tp4, int ft, int rot) {
  const unsigned q = lane >> 4, r16 = lane & 15;
  const int c = 7 * ft + wv;
  {
    f32x4 a;
#pragma unroll
    for (int k = 0; k < 4; ++k) a[k] = sum32(sum16(u2f(uv[0][k]) + u2f(uv[1][k])));
    if (lane < 16) *(LAS f32x4*)(L_UV + wv * 256 + lane * 16) = a;
  }
  const unsigned ub = L_UV + wv * 256 + q * 64;
  const f32x4 ug = lds_f4(ub), vg = lds_f4(ub + 16), uu = lds_f4(ub + 32), vu = lds_f4(ub + 48);
  const unsigned gvoff = (q & 1) * 57344 + (c >> 1) * 1024 + (c & 1) * 512 + (q >> 1) * 256 + r16 * 16;
  const unsigned sb = opv(L_ST + r16 * 8);
  unsigned ab[8];
#pragma unroll
  for (int g = 0; g < 8; ++g) ab[g] = lane * 16 + (unsigned)(((g + rot) & 7) * 2048);
  lds_wait_ge(SY_ADONE + 0, 1);
#pragma unroll 1
  for (int u = 0; u < 4; ++u) {
    const int tbl = (u + ft) & 3;
    STAMP(16 + 2 * u);
    f32x4 gx = {}, ux = {}, gy = {}, uy = {};
    bf16x8 A[32];
    auto rda = [&](int i) { A[i] = lds_frag(ab[i >> 2] + ((i >> 1) & 1) * 1024 + (i & 1) * 16384); };
    static_for<0, 6>([&](auto ic) { rda(decltype(ic)::value); });
    unsigned nxt, sto;
    SCHED();
    static_for<0, 32>([&](auto ic) {
      constexpr int i = decltype(ic)::value;
      if constexpr (i & 1) { gy = MFMA16(W[i - 1], A[i], gy); SCHED(); uy = MFMA16(W[i], A[i], uy); }
      else { gx = MFMA16(W[i], A[i], gx); SCHED(); ux = MFMA16(W[i + 1], A[i], ux); }
      SCHED();
      if constexpr (i + 6 < 32) rda(i + 6);
      if constexpr (i == 26) nxt = lds_get(SY_ADONE + u + 1);
      if constexpr (i == 27) sto = lds_get(SY_STAT);
      SCHED();
    });
    STAMP(17 + 2 * u);
    if (lane == 0) lds_inc(SY_UFIN + u);
    if (!RFL(sto)) lds_wait_ge(SY_STAT, 1);
    const f32x2 sx = *(const LAS f32x2*)(sb + tbl * 256), sy = *(const LAS f32x2*)(sb + tbl * 256 + 128);
#pragma unroll
    for (int g = 0; g < 8; ++g) ab[g] += 32768;
    float ox[4], oy[4];
#pragma unroll
    for (int j = 0; j < 4; ++j) {
      ox[j] = geglu(gx[j], ux[j], sx[0], sx[1], ug[j], vg[j], uu[j], vu[j]);
      oy[j] = geglu(gy[j], uy[j], sy[0], sy[1], ug[j], vg[j], uu[j], vu[j]);
    }
    __builtin_amdgcn_raw_buffer_store_b128(merge16(pk2(ox[0], ox[1]), pk2(ox[2], ox[3]), pk2(oy[0], oy[1]), pk2(oy[2], oy[3])), rws, gvoff,
                                           WS_G + (unsigned)(2 * (4 * tp4 + tbl)) * 57344u, 16);
    if (!RFL(nxt)) lds_wait_ge(SY_ADONE + u + 1, 1);
  }
  WAITV(0);
  unsigned old = 0;
  if (lane == 0) old = lds_inc(SY_GSIG);
  if (RFL(old) == 6 && lane < 4) cnt_add(ws + WS_CG + (4 * tp4 + lane) * 256);
}

#define C_XCD(b) ((b) & 7)
#define C_LOC(b) ((b) >> 3)
#define C_TP(b) (2 * C_XCD(b) + (C_LOC(b) >> 4))
#define C_PART(b) ((C_LOC(b) >> 3) & 1)
#define C_DN(b) (C_LOC(b) & 7)
#define C_J0(b) (4 * ((C_DN(b) + C_XCD(b) + 4 * C_PART(b)) & 7))
#define C_TB64(b) (4 * C_XCD(b) + (C_LOC(b) >> 3))
#define C_TP4(b) (8 * (C_XCD(b) & 1) + (C_LOC(b) >> 2))
#define C_FT(b) (4 * (C_XCD(b) >> 1) + (C_LOC(b) & 3))
#define C_ROT5(b) ((7 * C_DN(b) / 4 + 7 * (C_TB64(b) & 3) / 2) % 14)

DI unsigned side_producer(unsigned lane, int b) {
  const unsigned v = (lane >> 2) > 6 ? 6 : lane >> 2;
  const unsigned cbk = (unsigned)((0x18202820100800ull >> (8 * v)) & 0xff) + (v < 5 ? C_DN(b) : (b / 28) & 7);
  return lane < 28 ? cbk * 4 + (lane & 3) : (b & ~56) + ((lane + 4) & 7) * 8;
}

__global__ __launch_bounds__(512, 2) void fused_block(Params p) {
  const int b0 = blockIdx.x;
  const unsigned lane0 = threadIdx.x & 63;
  const int wv = RFL(threadIdx.x >> 6);
  char* const ws = p.ws;
  u64 pc0;
  asm volatile("s_getpc_b64 %0" : "=s"(pc0));
  STAMP(0);
  const u32x4 sws = mk_srd(ws, WS_BYTES);
  const __amdgpu_buffer_rsrc_t rws = mk_rsrc(ws, WS_BYTES);

  if (wv >= 4) {
    const int wl = wv - 4;
    const unsigned lane = lane0;
    const unsigned sw = (lane & 7) ^ (lane >> 4);
    const unsigned vl = lane * 16;
    {
      const int b = b0, j0 = C_J0(b), dn = C_DN(b);
      unsigned junk = 0;
      if (wv == 4 && lane < CODE_LINES_PER_WG)
        asm volatile("global_load_dword %0, %1, off" : "=&v"(junk) : "v"((const char*)(pc0 & ~255ull) + (C_LOC(b) * CODE_LINES_PER_WG + lane) * 128) : "memory");
      const u32x4 sA = mk_srd(C_PART(b) ? p.wsim : p.wsre, 2048u * 2048u * 2u);
      const unsigned ra = (unsigned)(128 * C_TP(b) + 32 * wl + (lane >> 3)) * 4096u;
      const unsigned va0 = ra + sw * 16, va1 = ra + 8 * 4096 + (sw ^ 4) * 16 - 1024, va2 = ra + 16 * 4096 + sw * 16 - 2048, va3 = ra + 24 * 4096 + (sw ^ 4) * 16 - 3072;
      const unsigned o1 = (lane & 11) == 0 ? 0x3f803f80u : 0u;
      const u32x4 ones4 = {o1, o1, o1, o1};
      const bf16x8 sel = __builtin_bit_cast(bf16x8, ones4);
      const unsigned wa = (32 * wl + (lane & 15)) * 128 + (((lane >> 4) ^ ((lane >> 1) & 7)) * 16);
      f32x4 r0 = {}, r1 = {};
      auto rsum = [&](int s) {
        const unsigned a = wa + s * L_STAGE;
        r0 = MFMA16(sel, lds_frag(a), r0); r1 = MFMA16(sel, lds_frag(a + 2048), r1);
        r0 = MFMA16(sel, lds_frag(a ^ 64), r0); r1 = MFMA16(sel, lds_frag((a ^ 64) + 2048), r1);
      };
      auto half = [&](int hs) {
        if (hs & 1) dma2n(sA, va2 + 2048, va3 + 2048, (unsigned)(j0 + (hs >> 1)) * 128u, (hs >> 1) * L_STAGE + wl * 4096 + 2048);
        else dma2n(sA, va0, va1, (unsigned)(j0 + (hs >> 1)) * 128u, (hs >> 1) * L_STAGE + wl * 4096);
      };
      half(0);
      BAR();
#pragma unroll
      for (int hs = 1; hs < 8; ++hs) {
        if (2 * hs > A_PACE) asm volatile("s_waitcnt vmcnt(%1)" : "+v"(junk) : "i"(A_PACE) : "memory");
        half(hs);
      }
      {
        const unsigned m0 = (unsigned)(128 * (b & 3) + 32 * wl + (lane >> 3)) * 6144u + (lane & 7) * 16 + (b < 192 ? (b >> 2) * 128 : 0x40000000);
        const u32x4 sM = mk_srd(p.wmod, 512u * 3072u * 2u);
        WAITV(4);
        dma2n(sM, m0, m0 + 8 * 6144 - 1024, 0, L_AUX + wl * 4096);
        rsum(0); rsum(1);
        WAITV(4);
        dma2n(sM, m0 + 16 * 6144, m0 + 24 * 6144 - 1024, 0, L_AUX + wl * 4096 + 2048);
        rsum(2);
        WAITL0();
      }
      STAMP(1);
      BAR();
      STAMP(2);
      BAR();
      STAMPB(1);
      auto stage1 = [&](int s) {
        const unsigned j = (unsigned)(j0 + s) & 31u, sl = (unsigned)(s & 3) * L_STAGE;
        WAITV(L_PACE);
        dma2c(sws, vl, WS_NT + ((unsigned)dn * 32u + j) * 8192u + wl * 2048, sl + 16384 + wl * 2048);
      };
      auto stage2 = [&](int s) {
        const unsigned j = (unsigned)(j0 + s) & 31u, sl = (unsigned)(s & 3) * L_STAGE;
        WAITV(L_PACE);
        dma2v(sA, va0, va1, j * 128u, sl + wl * 4096);
        WAITV(L_PACE);
        dma2v(sA, va2 + 2048, va3 + 2048, j * 128u, sl + wl * 4096 + 2048);
      };
      auto stage = [&](int s) { stage1(s); stage2(s); };
      stage(4);
      BAR();
      STAMPB(2);
      stage(5);
      WAITV(12); BAR();
      STAMPB(3);
      rsum(3);
      if (lane < 32) __builtin_amdgcn_raw_buffer_store_b32(f2u(lane < 16 ? r0[0] : r1[0]), rws, lane * 4, WS_RSP + (unsigned)((dn * 2 + C_PART(b)) * 2048 + 128 * C_TP(b) + 32 * wl) * 4u, 16);
      stage(6);
      WAITV(12); BAR();
      STAMPB(4);
#pragma unroll 1
      for (int s = 5; s < L_EARLY; ++s) { stage(s + 2); WAITV(12); BAR(); STAMPB(s); if ((s & 3) == 0) STAMP(23 + (s >> 2)); }
#pragma unroll 1
      for (int s = L_EARLY; s <= 29; ++s) { stage1(s + 2); WAITV(8); BAR(); STAMPB(s); if ((s & 3) == 0) STAMP(23 + (s >> 2)); stage2(s + 2); }
    }
    const int b = ops(b0);
    {
      const unsigned rd = (unsigned)(64 * C_DN(b) + 16 * wl + (lane >> 3)) * 1024u;
      const unsigned vd0 = rd + sw * 16, vd1 = rd + 8 * 1024 + (sw ^ 4) * 16 - 1024;
      auto wdblk = [&](int n, bool first) {
        const unsigned blk = (unsigned)(n + 4 * (C_TB64(b) & 3)) & 15u;
        const unsigned sl = (unsigned)(n < 7 ? 12 + n : n - 7) * 8192u + wl * 2048;
        const u32x4 sd = mk_srd(blk >= 8 ? p.wdim : p.wdre, 512u * 512u * 2u);
        if (first) dma2n(sd, vd0, vd1, (blk & 7u) * 128u, sl); else dma2v(sd, vd0, vd1, (blk & 7u) * 128u, sl);
      };
      WAITV(6); BAR();
      STAMPB(30);
      WAITV(4); wdblk(0, true); WAITV(4); wdblk(1, true);
      WAITV(4); BAR();
      STAMP(3);
      wdblk(2, true); WAITV(WD_PACE); wdblk(3, true);
#pragma unroll 1
      for (int n = 4; n < 16; ++n) { WAITV(WD_PACE); wdblk(n, false); if ((n & 3) == 0) STAMP(9 + (n >> 2)); }
    }
    const int tp4 = C_TP4(b), ft = C_FT(b), rot4 = C_LOC(b) >> 2;
    if (wv < 7) {
      bf16x8 W[32];
      u32x4 uv[2];
      WAITV(0);
      lds_set(SY_WD + wl, 1);
      STAMP(4);
      if (wv >= W_EARLY) BAR();
      lds_wait_ge(SY_AUX, 1);
      load_w(W, uv, rws, 7 * ft + wv, rot4, lane);
      if (wv < W_EARLY) BAR();
      STAMP(5);
      k4_body(W, uv, rws, ws, wv, lane, tp4, ft, rot4);
      STAMP(8);
    } else {
      WAITV(0);
      lds_set(SY_WD + 3, 1);
      STAMP(4);
      {
        const unsigned ep = RFL(lds_get(SY_EP));
        const char* fp = ws + WS_AUXFLAG + (lane < 56 ? lane : 55u) * 16;
_Pragma("unroll 1") for (int sp = 0; sp < SPIN_CAP; ++sp) {
          u32x4 f0;
          asm volatile("global_load_dwordx4 %0, %1, off sc1\n\ts_waitcnt vmcnt(0)" : "=&v"(f0) : "v"(fp) : "memory");
          if (all_lanes(f0[0] == ep && f0[1] == ep && f0[2] == ep && f0[3] == ep)) break;
          __builtin_amdgcn_s_sleep(16);
        }
        lds_set(SY_AUX, 1);
        STAMP(9);
      }
      const unsigned baseAP = lds_get(SY_BAP + (lane & 3));
      BAR();
      STAMP(5);
      const int rot5 = C_ROT5(b), et = C_DN(b);
      auto wdn = [&](int n0, int n1) {
#pragma unroll 1
        for (int n = n0; n < n1; ++n) {
          int kb = (n >> 1) + rot5; kb = (kb >= 14 ? kb - 14 : kb) + 14 * (n & 1);
          int sn = n + 16; sn = sn >= 38 ? sn - 38 : sn >= 19 ? sn - 19 : sn;
          const unsigned so = WS_WDT + (unsigned)(et * 28 + kb) * 8192u, sl = (unsigned)sn * 8192u;
          dma4c(sws, vl, so, sl); dma4c(sws, vl, so + 4096, sl + 4096);
          WAITV(8);
        }
      };
      cnt_wait(ws + WS_CAP + (4 * tp4 + lane) * 256, baseAP, lane, 4);
      STAMP(6);
      auto ablk = [&](int h0, int h1) {
#pragma unroll 1
        for (int hh = h0; hh < h1; ++hh) {
          const unsigned so = WS_AP + (unsigned)(4 * tp4 + (((hh >> 1) + ft) & 3)) * 32768u + (hh & 1) * 16384, sl = (unsigned)hh * 16384u;
#pragma unroll
          for (int q = 0; q < 4; ++q) dma4c(sws, vl, so + q * 4096, sl + q * 4096);
        }
      };
      ablk(0, 3);
      WAITV(16); lds_set(SY_ADONE + 0, 1);
      STAMP(7);
      ablk(3, 4);
      u32x2 sp[2][8];
#pragma unroll
      for (int hf = 0; hf < 2; ++hf)
#pragma unroll
        for (int e = 0; e < 8; ++e)
          sp[hf][e] = __builtin_amdgcn_raw_buffer_load_b64(rws, lane * 8 + hf * 512, WS_STATS + (unsigned)(e * 2048 + 128 * tp4) * 8u, 16);
      CFENCE();
      WAITV(16); lds_set(SY_ADONE + 1, 1);
      STAMP(16);
#pragma unroll
      for (int hf = 0; hf < 2; ++hf) {
        float mu = 0.f, m2 = 0.f;
#pragma unroll
        for (int e = 0; e < 8; ++e) { mu += u2f(sp[hf][e][0]); m2 += u2f(sp[hf][e][1]); }
        mu *= 0.125f;
        float dv = 0.f;
#pragma unroll
        for (int e = 0; e < 8; ++e) { const float d = u2f(sp[hf][e][0]) - mu; dv = fmaf(d, d, dv); }
        const float rstd = __builtin_amdgcn_rsqf(fmaf(dv, 64.f, m2) * (1.f / 512.f) + 1e-5f);
        const f32x2 o = {rstd, -mu * rstd};
        *(LAS f32x2*)(L_ST + (64 * hf + lane) * 8) = o;
      }
      WAITL0(); lds_set(SY_STAT, 1);
      STAMP(17);
      ablk(4, 7);
      WAITV(16); lds_set(SY_ADONE + 2, 1);
      STAMP(18);
      ablk(7, 8);
      WAITV(0); lds_set(SY_ADONE + 3, 1);
      STAMP(19);
      wdn(0, 3);
      lds_wait_ge(SY_UFIN + 0, 7); wdn(3, 7);
      lds_wait_ge(SY_UFIN + 1, 7); wdn(7, 11);
      lds_set(SY_WL + 0, 1);
      STAMP(20);
      lds_wait_ge(SY_UFIN + 2, 7); wdn(11, 15);
      lds_wait_ge(SY_UFIN + 3, 7); wdn(15, 19);
      WAITV(0); lds_set(SY_WL + 1, 1);
      STAMP(8);
      lds_wait_ge(SY_PROG + 0, 4);
      wdn(19, 27);
      lds_set(SY_WL + 2, 1);
      STAMP(21);
      lds_wait_ge(SY_PROG + 1, 4);
      wdn(27, 28);
      WAITV(0); lds_set(SY_WL + 3, 1);
      STAMP(22);
    }
  } else {
    const int w = wv;
    unsigned epoch, basePQ, baseG;
    {
      const int b = b0;
      const unsigned lane = lane0, q = lane >> 4, r16 = lane & 15;
      u32x4 xa, xb;
      asm volatile("global_load_dwordx4 %0, %2, off nt\n\tglobal_load_dwordx4 %1, %2, off offset:1024 nt" : "=&v"(xa), "=&v"(xb) : "v"((const char*)p.x + (8 * b + 2 * w) * 1024 + lane * 16) : "memory");
      unsigned sv = 0, ev;
      asm volatile("global_load_dword %0, %1, off sc1" : "=&v"(ev) : "v"(ws + WS_NONCE) : "memory");
      {
        const unsigned off = lane < 2 ? (lane ? WS_CG : WS_CPQ) + (2 * C_TB64(b) + (w >> 1)) * 256 : WS_CAP + (4 * C_TP4(b) + lane - 2) * 256;
        if (lane < (w == 0 ? 6u : 2u)) asm volatile("global_atomic_add %0, %1, %2, off sc0" : "=&v"(sv) : "v"(ws + off), "v"(0u) : "memory");
      }
      if (threadIdx.x < SY_N) lds_set(threadIdx.x, 0);
      WAITL0(); BAR();
      __builtin_amdgcn_s_setprio(1);
      asm volatile("s_waitcnt vmcnt(2)" : "+v"(xa), "+v"(xb)::"memory");
      STAMP(13);
      {
        float va[8], vb[8], sa = 0.f, sb = 0.f;
#pragma unroll
        for (int i = 0; i < 4; ++i) { va[2 * i] = bflo(xa[i]); va[2 * i + 1] = bfhi(xa[i]); vb[2 * i] = bflo(xb[i]); vb[2 * i + 1] = bfhi(xb[i]); }
#pragma unroll
        for (int i = 0; i < 8; ++i) { sa += va[i]; sb += vb[i]; }
        sa = wave_sum(sa) * (1.f / 512.f); sb = wave_sum(sb) * (1.f / 512.f);
        float qa = 0.f, qb = 0.f;
#pragma unroll
        for (int i = 0; i < 8; ++i) { va[i] -= sa; vb[i] -= sb; qa = fmaf(va[i], va[i], qa); qb = fmaf(vb[i], vb[i], qb); }
        qa = __builtin_amdgcn_rsqf(wave_sum(qa) * (1.f / 512.f) + 1e-5f); qb = __builtin_amdgcn_rsqf(wave_sum(qb) * (1.f / 512.f) + 1e-5f);
        const u32x4 na = {pk2(va[0] * qa, va[1] * qa), pk2(va[2] * qa, va[3] * qa), pk2(va[4] * qa, va[5] * qa), pk2(va[6] * qa, va[7] * qa)};
        const u32x4 nb = {pk2(vb[0] * qb, vb[1] * qb), pk2(vb[2] * qb, vb[3] * qb), pk2(vb[4] * qb, vb[5] * qb), pk2(vb[6] * qb, vb[7] * qb)};
        *(LAS u32x4*)(L_LN + (2 * w) * 1088 + lane * 16) = na;
        *(LAS u32x4*)(L_LN + (2 * w + 1) * 1088 + lane * 16) = nb;
      }
      CFENCE();
      if (lane == 0) lds_inc(SY_LN);
      lds_wait_ge(SY_LN, 4);
      {
        const unsigned la = L_LN + (r16 >> 2) * 1088 + (128 * w + 16 * q + 4 * (lane & 3)) * 2;
#pragma unroll
        for (int i = 0; i < 2; ++i) {
          const bf16x8 f = lds_tr2(la + i * 128, la + i * 128 + 4 * 1088);
          __builtin_amdgcn_raw_buffer_store_b128(__builtin_bit_cast(u32x4, f), rws, lane * 16, WS_NT + (unsigned)((2 * w + i) * 256 + b) * 1024u, 16);
        }
      }
      STAMP(14);
      asm volatile("s_waitcnt vmcnt(0)" : "+v"(sv), "+v"(ev)::"memory");
      basePQ = __builtin_amdgcn_readlane(sv, 0); baseG = __builtin_amdgcn_readlane(sv, 1);
      epoch = RFL(ev) + 1u;
      unsigned old = 0;
      if (lane == 0) old = lds_inc(SY_ND);
      if (RFL(old) == 3 && lane < 16) flag_st(ws + WS_NFLAG + lane * 1024 + (((b >> 1) & 3) * 64 + (b >> 3) * 2 + (b & 1)) * 4, epoch);
      STAMP(1);
      if (w == 0) { if (lane >= 2 && lane < 6) lds_set(SY_BAP + lane - 2, sv); if (lane == 6) lds_set(SY_EP, epoch); }
      {
        const char* fp = ws + WS_NFLAG + ((b >> 3) & 15) * 1024 + (w * 64 + lane) * 4;
_Pragma("unroll 1") for (int sp = 0; sp < SPIN_CAP; ++sp)
          if (all_lanes(atomic_peek(fp) == epoch)) break;
      }
#pragma unroll
      for (int s = 0; s < 4; ++s) dma2c(sws, lane * 16, WS_NT + (unsigned)(C_DN(b) * 32 + C_J0(b) + s) * 8192u + w * 2048, s * L_STAGE + 16384 + w * 2048);
      STAMP(2);
    }

    {
      f32x4 acc[2][4] = {};
      bf16x8 N[2][8], Wf[2][4];
      const unsigned nb0 = (lane0 >> 4) * 1024 + (lane0 & 15) * 16 + 16384;
      const unsigned wa0 = (32 * w + (lane0 & 15)) * 128 + (((lane0 >> 4) ^ ((lane0 >> 1) & 7)) * 16);
      const unsigned nbA = opv(nb0), nbB = opv(nb0 + 2 * L_STAGE), waA = opv(wa0), waB = opv(wa0 + 2 * L_STAGE), wbA = opv(wa0 ^ 64), wbB = opv((wa0 ^ 64) + 2 * L_STAGE);
      auto rd = [&](int s) {
        const unsigned o = (s & 1) * L_STAGE;
        const unsigned nb = (s & 2) ? nbB : nbA, wa = (s & 2) ? waB : waA, wb = (s & 2) ? wbB : wbA;
        Wf[s & 1][0] = lds_frag(wa + o); Wf[s & 1][1] = lds_frag(wa + o + 2048);
#pragma unroll
        for (int i = 0; i < 4; ++i) N[s & 1][i] = lds_frag(nb + o + i * 256);
        Wf[s & 1][2] = lds_frag(wb + o); Wf[s & 1][3] = lds_frag(wb + o + 2048);
#pragma unroll
        for (int i = 0; i < 4; ++i) N[s & 1][4 + i] = lds_frag(nb + o + 4096 + i * 256);
      };
      auto mm = [&](int s) {
#pragma unroll
        for (int kk = 0; kk < 2; ++kk)
#pragma unroll
          for (int i = 0; i < 4; ++i) {
            acc[0][i] = MFMA16(N[s & 1][4 * kk + i], Wf[s & 1][2 * kk], acc[0][i]);
            acc[1][i] = MFMA16(N[s & 1][4 * kk + i], Wf[s & 1][2 * kk + 1], acc[1][i]);
          }
      };
      WAITV(6); BAR();
      STAMP(3);
      rd(0);
      WAITL0();
      WAITV(4);
      static_for<1, 32>([&](auto sc) {
        constexpr int s = decltype(sc)::value;
        SCHED(); BAR(); SCHED();
        rd(s);
        mm(s - 1);
#pragma unroll
        for (int i = 0; i < 12; ++i) { SGB(0x8, 1); SGB(0x100, 1); }
        SGB(0x8, 4);
        SCHED();
        WAITL0();
        SCHED();
        if constexpr (s == 1) {
          WAITV(2);
        } else if constexpr (s == 2) {
          WAITV(0);
        } else if constexpr (s == 3) {
          const int b = ops(b0);
          if (b == 0 && w == 0 && lane0 == 0) flag_st(ws + WS_NONCE, epoch);
          dma1w(mk_srd(p.c, 1024u), opv(lane0) * 4, (b & 3) * 256, L_HK + w * HK);
        } else if constexpr (s == 5) {
          const int b = ops(b0);
          const unsigned lane = opv(lane0), q = lane >> 4, r16 = lane & 15;
          WAITV(0);
          LAS unsigned* cp = (LAS unsigned*)(L_HK + w * HK + lane * 4);
          const unsigned cc = *cp;
          const float c0 = bflo(cc), c1 = bfhi(cc);
          *cp = pk2(c0 * __builtin_amdgcn_rcpf(1.f + __builtin_amdgcn_exp2f(c0 * -1.44269504f)), c1 * __builtin_amdgcn_rcpf(1.f + __builtin_amdgcn_exp2f(c1 * -1.44269504f)));
          f32x4 d = {};
          const unsigned ma = L_AUX + (8 * q + (r16 >> 2)) * 128 + (16 * w + 4 * (lane & 3)) * 2;
#pragma unroll
          for (int k = 0; k < 4; ++k) d = MFMA16(lds_frag(L_HK + w * HK + (32 * k + 8 * q) * 2), lds_tr2(ma + k * 4096, ma + k * 4096 + 512), d);
          if (b < 192 && lane < 16) __builtin_amdgcn_raw_buffer_store_b32(f2u(d[0]), rws, lane * 4, WS_MODP + (unsigned)((b & 3) * 3072 + (b >> 2) * 64 + 16 * w) * 4u, 16);
          WAITL0();
        } else if constexpr (s == 7) {
          WAITV(0);
        } else if constexpr (s == 8) {
          if (w == 0 && lane0 == 0) flag_st(ws + WS_MODFLAG + ops(b0) * 4, epoch);
        } else if constexpr (s == AUXL || s == AUXL + 2) {
          const int b = ops(b0);
          const unsigned lane = opv(lane0);
          if (b < 224) {
            const unsigned g0 = (unsigned)(64 * (b / 28) + 16 * w + (lane >> 3)) * 3584u + (lane & 7) * 16;
            dma2n(mk_srd(s == AUXL ? p.wg : p.wu, 512u * 1792u * 2u), g0, g0 + 8 * 3584 - 1024, (b % 28) * 128, L_AUX + (s == AUXL ? 0 : 8192) + w * 2048);
          }
        } else if constexpr (s == AUXL + 4) {
          const int b = ops(b0);
          const unsigned lane = opv(lane0);
          if (b < 224) {
            const unsigned d0 = (unsigned)(64 * (b >> 3) + 16 * w + (lane >> 3)) * 1024u + (lane & 7) * 16;
            dma2n(mk_srd(p.wdn, 1792u * 512u * 2u), d0, d0 + 8 * 1024 - 1024, (b & 7) * 128, L_AUX + 16384 + w * 2048);
          }
        } else if constexpr (s == 12) {
          dma1w(sws, side_producer(opv(lane0), ops(b0)) * 4, WS_MODFLAG, L_HK + w * HK);
        } else if constexpr (s == 15) {
          const int b = ops(b0);
          const unsigned lane = opv(lane0);
          const int akb = (b / 28) & 7, dn = C_DN(b);
          WAITV(0);
          if (!all_lanes(*(volatile LAS unsigned*)(L_HK + w * HK + lane * 4) == epoch)) {
            const unsigned pr = side_producer(lane, b);
_Pragma("unroll 1") for (int sp = 0; sp < SPIN_CAP; ++sp)
            {
              if (all_lanes(flag_ld(ws + WS_MODFLAG + pr * 4) == epoch)) break;
              __builtin_amdgcn_s_sleep(16);
            }
          }
          CFENCE();
          const int cb0 = (w == 3 ? 32 : 8 * w) + dn;
          const int cb1 = w == 0 ? 40 + dn : w == 1 ? 32 + akb : 24 + akb;
#pragma unroll
          for (int k = 0; k < 4; ++k) {
            dma1w(sws, lane * 4, WS_MODP + (unsigned)(k * 3072 + cb0 * 64) * 4u, L_HK + w * HK + 256 + k * 256);
            dma1w(sws, lane * 4, WS_MODP + (unsigned)(k * 3072 + cb1 * 64) * 4u, L_HK + w * HK + 1280 + k * 256);
            dma1w(sws, (lane >> 5) * 16384 + (lane & 31) * 4, WS_RSP + (unsigned)((4 * k + C_PART(b)) * 2048 + 128 * C_TP(b) + 32 * w) * 4u, L_HK + w * HK + 2304 + k * 256);
          }
        } else if constexpr (s == 17) {
          const unsigned lane = opv(lane0);
          WAITV(0);
          const LAS float* mp = (const LAS float*)(L_HK + w * HK + 256 + lane * 4);
          float m0 = rbf((mp[0] + mp[64]) + (mp[128] + mp[192])), m1 = rbf((mp[256] + mp[320]) + (mp[384] + mp[448]));
          const float rs = sum32((mp[512] + mp[576]) + (mp[640] + mp[704]));
          if (w & 1) m0 = rbf(1.f + m0);
          if (w == 1) m1 = rbf(1.f + m1);
          if (w < 2 && C_PART(ops(b0))) m0 = -m0;
          *(LAS float*)(L_MOD + w * 256 + lane * 4) = m0;
          if (w == 0) *(LAS float*)(L_MOD + 1024 + lane * 4) = m1;
          else if (w < 3) *(LAS u16*)(L_VB + (w - 1) * 128 + lane * 2) = (u16)(f2u(m1) >> 16);
          if (lane < 32) *(LAS float*)(L_RS + (32 * w + lane) * 4) = rs;
          WAITL0();
        } else if constexpr (s == 19) {
          const int b = ops(b0);
          if (b < 224) {
            const unsigned lane = opv(lane0), q = lane >> 4, r16 = lane & 15;
            const int akb = b / 28, acb = b % 28;
            f32x4 d[2] = {};
            const unsigned ga = L_AUX + (8 * q + (r16 >> 2)) * 128 + (16 * w + 4 * (lane & 3)) * 2;
#pragma unroll
            for (int k = 0; k < 4; ++k) {
              const unsigned a = ga + (k >> 1) * 4096 + (k & 1) * 8192;
              const bf16x8 f = lds_tr2(a, a + 512);
              __builtin_amdgcn_raw_buffer_store_b128(__builtin_bit_cast(u32x4, f), rws, lane * 16 + k * 1024, WS_WT + (unsigned)((4 * acb + w) * 32 + 4 * akb) * 1024u, 16);
              d[k & 1] = MFMA16(f, lds_frag(L_VB + (lane & 1) * 128 + (32 * (k >> 1) + 8 * q) * 2), d[k & 1]);
            }
            if (r16 < 2) { *(LAS f32x4*)(L_HK + w * HK + q * 64 + r16 * 16) = d[0]; *(LAS f32x4*)(L_HK + w * HK + q * 64 + 32 + r16 * 16) = d[1]; }
            WAITL0();
          }
        } else if constexpr (s == 20) {
          const int b = ops(b0);
          if (b < 224) {
            const unsigned lane = opv(lane0), q = lane >> 4, r16 = lane & 15;
            const unsigned ga = L_AUX + 16384 + (8 * q + (r16 >> 2)) * 128 + (16 * w + 4 * (lane & 3)) * 2;
#pragma unroll
            for (int k = 0; k < 2; ++k) {
              const bf16x8 f = lds_tr2(ga + k * 4096, ga + k * 4096 + 512);
              __builtin_amdgcn_raw_buffer_store_b128(__builtin_bit_cast(u32x4, f), rws, lane * 16, WS_WDT + (unsigned)(((b & 7) * 28 + (b >> 3)) * 8 + 4 * k + w) * 1024u, 16);
            }
            const f32x4 o = lds_f4(L_HK + w * HK + r16 * 16);
            if (lane < 16) __builtin_amdgcn_raw_buffer_store_b128(__builtin_bit_cast(u32x4, o), rws, lane * 16, WS_UVP + (unsigned)((b / 28) * 112 + 4 * (b % 28) + w) * 256u, 16);
            WAITL0();
          }
        } else if constexpr (s == AUXW) {
          WAITV(0);
        } else if constexpr (s == AUXW + 1) {
          const int b = ops(b0);
          if (b < 224 && w == 0 && lane0 == 0) flag_st(ws + WS_AUXFLAG + b * 4, epoch);
        }
      });
      const unsigned lane = opv(lane0), q = lane >> 4, r16 = lane & 15;
      const unsigned tb = opv(L_MOD + q * 16);
      f32x4 sa[4], c1[4];
#pragma unroll
      for (int i = 0; i < 4; ++i) { sa[i] = lds_f4(tb + i * 64); c1[i] = lds_f4(tb + 256 + i * 64); }
      const float rs0 = *(const LAS float*)(L_RS + (32 * w + r16) * 4), rs1 = *(const LAS float*)(L_RS + (32 * w + 16 + r16) * 4);
      SCHED();
      mm(31);
      SCHED();
      STAMP(4);
      const int b = ops(b0);
      const unsigned vo = ((q & 1) * 2 + (q >> 1)) * 256 + r16 * 16;
      const unsigned so = WS_PQ + (unsigned)((8 * C_TP(b) + 2 * w) * 32 + C_PART(b) * 16 + 2 * C_DN(b)) * 1024u;
#pragma unroll
      for (int tt = 0; tt < 2; ++tt) {
        const float rs = tt ? rs1 : rs0;
        unsigned pk[8];
#pragma unroll
        for (int i = 0; i < 4; ++i) {
          pk[2 * i] = pk2(fmaf(acc[tt][i][0], c1[i][0], rs * sa[i][0]), fmaf(acc[tt][i][1], c1[i][1], rs * sa[i][1]));
          pk[2 * i + 1] = pk2(fmaf(acc[tt][i][2], c1[i][2], rs * sa[i][2]), fmaf(acc[tt][i][3], c1[i][3], rs * sa[i][3]));
        }
#pragma unroll
        for (int pr = 0; pr < 2; ++pr)
          __builtin_amdgcn_raw_buffer_store_b128(merge16(pk[4 * pr], pk[4 * pr + 1], pk[4 * pr + 2], pk[4 * pr + 3]), rws, vo + pr * 1024, so + tt * 32768, 16);
      }
      WAITV(0);
      if (lane == 0) cnt_add(ws + WS_CPQ + (4 * C_TP(b) + w) * 256);
      STAMP(5);
    }

    float x1[16];
    {
      const int b = ops(b0);
      const unsigned lane = opv(lane0), q = lane >> 4, r16 = lane & 15, vl = lane * 16;
      const int tb64 = C_TB64(b), et = C_DN(b), tb16 = 4 * tb64 + w, t32c = 2 * tb64 + (w >> 1);
      u32x2 xr[4];
      {
        const __amdgpu_buffer_rsrc_t rx = mk_rsrc(p.x, 2048u * 512u * 2u);
#pragma unroll
        for (int e = 0; e < 4; ++e) xr[e] = __builtin_amdgcn_raw_buffer_load_b64(rx, r16 * 1024 + q * 8 + e * 32, (unsigned)(16 * tb16) * 1024u + et * 128, 2);
      }
      cnt_wait(ws + WS_CPQ + t32c * 256, basePQ, lane, 1);
      STAMP(6);
      const int kh = w & 1;
      bf16x8 pq[32];
#pragma unroll
      for (int n = 0; n < 32; ++n)
        pq[n] = __builtin_bit_cast(bf16x8, __builtin_amdgcn_raw_buffer_load_b128(rws, vl + ((n >> 1) & 1) * 1024, WS_PQ + (unsigned)((2 * t32c + (n & 1)) * 32 + 16 * kh + (((n >> 2) + et) & 7) * 2) * 1024u, 16));
      SCHED();
      lds_wait4(SY_WD);
      STAMP(15);
      f32x4 y2[2][4] = {};
      const unsigned la = r16 * 128 + ((q ^ (r16 >> 1)) * 16);
      bf16x8 F[64];
      unsigned fa[2];
      auto rdw = [&](int i) {
        if ((i & 7) == 0) {
          int nl = ((((i >> 3) + et) & 7) + 8 * kh - 4 * (tb64 & 3)) & 15;
          nl = nl >= 7 ? nl - 7 : nl + 12;
          fa[0] = opv(la + (unsigned)nl * 8192u); fa[1] = opv((la ^ 64u) + (unsigned)nl * 8192u);
        }
        F[i] = lds_frag(fa[(i >> 2) & 1] + (i & 3) * 2048);
      };
      static_for<0, 6>([&](auto ic) { rdw(decltype(ic)::value); });
      SCHED();
      static_for<0, 64>([&](auto ic) {
        constexpr int i = decltype(ic)::value;
        y2[0][i & 3] = MFMA16(F[i], pq[2 * (i >> 2)], y2[0][i & 3]);
        SCHED();
        y2[1][i & 3] = MFMA16(F[i], pq[2 * (i >> 2) + 1], y2[1][i & 3]);
        SCHED();
        if constexpr (i + 6 < 64) rdw(i + 6);
        SCHED();
      });
      f32x4 y[4];
      pair_reduce(y, y2, 9 * 8192, SY_X3, w, lane);
      STAMP(7);
      const unsigned tb = opv(L_MOD + 512 + q * 16);
      f32x4 ga[4], c1[4];
#pragma unroll
      for (int e = 0; e < 4; ++e) { ga[e] = lds_f4(tb + e * 64); c1[e] = lds_f4(tb + 256 + e * 64); }
      unsigned pk[8];
      float sm = 0.f;
#pragma unroll
      for (int e = 0; e < 4; ++e) {
        x1[4 * e] = fmaf(ga[e][0], y[e][0], bflo(xr[e][0])); x1[4 * e + 1] = fmaf(ga[e][1], y[e][1], bfhi(xr[e][0]));
        x1[4 * e + 2] = fmaf(ga[e][2], y[e][2], bflo(xr[e][1])); x1[4 * e + 3] = fmaf(ga[e][3], y[e][3], bfhi(xr[e][1]));
        pk[2 * e] = pk2(x1[4 * e] * c1[e][0], x1[4 * e + 1] * c1[e][1]); pk[2 * e + 1] = pk2(x1[4 * e + 2] * c1[e][2], x1[4 * e + 3] * c1[e][3]);
        sm += (x1[4 * e] + x1[4 * e + 1]) + (x1[4 * e + 2] + x1[4 * e + 3]);
      }
      const unsigned vo = ((q & 1) * 2 + (q >> 1)) * 256 + r16 * 16;
#pragma unroll
      for (int pr = 0; pr < 2; ++pr)
        __builtin_amdgcn_raw_buffer_store_b128(merge16(pk[4 * pr], pk[4 * pr + 1], pk[4 * pr + 2], pk[4 * pr + 3]), rws, vo + pr * 1024, WS_AP + (unsigned)(tb16 * 16 + 2 * et) * 1024u, 16);
      const float mu = sum32(sum16(sm)) * (1.f / 64.f);
      float m2 = 0.f;
#pragma unroll
      for (int i = 0; i < 16; ++i) { const float d = x1[i] - mu; m2 = fmaf(d, d, m2); }
      m2 = sum32(sum16(m2));
      if (lane < 16) {
        const u32x2 o = {f2u(mu), f2u(m2)};
        __builtin_amdgcn_raw_buffer_store_b64(o, rws, lane * 8, WS_STATS + (unsigned)(et * 2048 + 16 * tb16) * 8u, 16);
      }
      WAITL0();
      BAR();
      WAITV(0);
      if (lane == 0) cnt_add(ws + WS_CAP + t32c * 256);
      STAMP(8);
    }
    __builtin_amdgcn_s_setprio(0);
    {
      const int b = ops(b0);
      const unsigned lane = opv(lane0);
      bf16x8 W[32];
      u32x4 uv[2];
      lds_wait_ge(SY_AUX, 1);
      load_w(W, uv, rws, 7 * C_FT(b) + w, C_LOC(b) >> 2, lane);
      k4_body(W, uv, rws, ws, w, lane, C_TP4(b), C_FT(b), C_LOC(b) >> 2);
    }
    STAMP(9);

    {
      const int b = ops(b0);
      const unsigned lane = opv(lane0), q = lane >> 4, r16 = lane & 15, vl = lane * 16;
      const int tb64 = C_TB64(b), et = C_DN(b), tb16 = 4 * tb64 + w, t32c = 2 * tb64 + (w >> 1);
      cnt_wait(ws + WS_CG + t32c * 256, baseG, lane, 1);
      STAMP(10);
      const int kh = w & 1, rot5 = C_ROT5(b);
      const unsigned gb0 = WS_G + (unsigned)(2 * t32c) * 57344u + (unsigned)kh * 28672u;
      const unsigned vy = opv(vl + 57344);
      bf16x8 g[56];
      auto ldg = [&](int m) {
        int kb = m + rot5; kb = kb >= 14 ? kb - 14 : kb;
#pragma unroll
        for (int i = 0; i < 4; ++i)
          g[4 * m + i] = __builtin_bit_cast(bf16x8, __builtin_amdgcn_raw_buffer_load_b128(rws, ((i & 1) ? vy : vl) + (i >> 1) * 1024, gb0 + (unsigned)kb * 2048u, 16));
      };
      static_for<0, G_WIN>([&](auto mc) { ldg(decltype(mc)::value); });
      f32x4 y2[2][4] = {};
      bf16x8 F[112];
      unsigned fa;
      auto rdw = [&](int i) {
        if ((i & 7) == 0) {
          const int m = i >> 3;
          if (m == 5) lds_wait_ge(SY_WL + 1, 1);
          if (m == 9) lds_wait_ge(SY_WL + 2, 1);
          if (m == 13) lds_wait_ge(SY_WL + 3, 1);
          const int s0 = (16 + 2 * m) % 19, s1 = (17 + 2 * m) % 19;
          fa = opv(vl + (unsigned)(kh ? s1 : s0) * 8192u);
        }
        F[i] = lds_frag(fa + (i & 7) * 1024);
      };
      lds_wait_ge(SY_WL + 0, 1);
      static_for<0, 6>([&](auto ic) { rdw(decltype(ic)::value); });
      SCHED();
      static_for<0, 112>([&](auto ic) {
        constexpr int i = decltype(ic)::value;
        y2[0][i & 3] = MFMA16(F[i], g[2 * (i >> 2)], y2[0][i & 3]);
        SCHED();
        y2[1][i & 3] = MFMA16(F[i], g[2 * (i >> 2) + 1], y2[1][i & 3]);
        SCHED();
        if constexpr (i + 6 < 112) rdw(i + 6);
        if constexpr ((i & 7) == 7 && (i >> 3) + G_WIN < 14) ldg((i >> 3) + G_WIN);
        if constexpr (i == 31 || i == 47) { if (lane == 0) lds_inc(SY_PROG + (i == 47)); }
        if constexpr (i == 31 || i == 55 || i == 87) STAMP(24 + (i >> 5) + (i == 55));
        SCHED();
      });
      lds_wait_ge(SY_PROG + 1, 4);
      f32x4 y[4];
      pair_reduce(y, y2, 6 * 8192, SY_X5, w, lane);
      STAMP(11);
      const unsigned tb = opv(L_MOD + 1024 + q * 16);
      unsigned pk[8];
#pragma unroll
      for (int e = 0; e < 4; ++e) {
        const f32x4 gb = lds_f4(tb + e * 64);
        pk[2 * e] = pk2(fmaf(gb[0], y[e][0], x1[4 * e]), fmaf(gb[1], y[e][1], x1[4 * e + 1]));
        pk[2 * e + 1] = pk2(fmaf(gb[2], y[e][2], x1[4 * e + 2]), fmaf(gb[3], y[e][3], x1[4 * e + 3]));
      }
      const __amdgpu_buffer_rsrc_t ro = mk_rsrc(p.out, 2048u * 512u * 2u);
#pragma unroll
      for (int pr = 0; pr < 2; ++pr)
        __builtin_amdgcn_raw_buffer_store_b128(merge16(pk[4 * pr], pk[4 * pr + 1], pk[4 * pr + 2], pk[4 * pr + 3]), ro, r16 * 1024 + (q & 1) * 32 + (q >> 1) * 16 + pr * 64,
                                               (unsigned)(16 * tb16) * 1024u + et * 128, 0);
    }
    STAMP(12);
  }
}

#if defined(__HIP_DEVICE_COMPILE__)
#pragma clang attribute pop
#endif

extern "C" void kernel_launch(void* const* d_in, const int* in_sizes, int n_in, void* d_out, int out_size, void* d_ws, size_t ws_size,
                              hipStream_t stream) {
  Params p;
  p.x = (const u16*)d_in[0]; p.wsre = (const u16*)d_in[1]; p.wsim = (const u16*)d_in[2];
  p.wdre = (const u16*)d_in[3]; p.wdim = (const u16*)d_in[4];
  p.wg = (const u16*)d_in[6]; p.wu = (const u16*)d_in[7]; p.wdn = (const u16*)d_in[8];
  p.c = (const u16*)d_in[9]; p.wmod = (const u16*)d_in[10];
  p.out = (u16*)d_out; p.ws = (char*)d_ws;
  (void)hipFuncSetAttribute((const void*)fused_block, hipFuncAttributeMaxDynamicSharedMemorySize, LDS_BYTES);
  fused_block<<<256, 512, LDS_BYTES, stream>>>(p);
}
```

```cpp
#define PROBE 13
#include <hip/hip_runtime.h>

typedef unsigned short u16;
typedef unsigned long long u64;
typedef short bf16x8 __attribute__((ext_vector_type(8)));
typedef short s16x4 __attribute__((ext_vector_type(4)));
typedef __bf16 bf2 __attribute__((ext_vector_type(2)));
typedef float f32x16 __attribute__((ext_vector_type(16)));
typedef float f32x4 __attribute__((ext_vector_type(4)));
typedef float f32x2 __attribute__((ext_vector_type(2)));
typedef unsigned u32x4 __attribute__((ext_vector_type(4)));
typedef unsigned u32x2 __attribute__((ext_vector_type(2)));
#define DI __device__ __forceinline__
#define LAS __attribute__((address_space(3)))
#define GAS __attribute__((address_space(1)))

constexpr unsigned WS_NONCE = 0;
constexpr unsigned WS_FLAG = 131072;
constexpr unsigned WS_CPQ = 16384;
constexpr unsigned WS_CAP = 32768;
constexpr unsigned WS_CG = 49152;
constexpr unsigned WS_MODP = 65536;
constexpr unsigned WS_STATS = 393216;
constexpr unsigned WS_RSP = 524288;
constexpr unsigned WS_NT = 1u << 20;
constexpr unsigned WS_PQ = 4u << 20;
constexpr unsigned WS_AP = 8u << 20;
constexpr unsigned WS_G = 10u << 20;
constexpr unsigned WS_WT = 18u << 20;
constexpr unsigned WS_WDT = 22u << 20;
constexpr unsigned WS_WDH = 24u << 20;
constexpr unsigned WS_DIAG = 25u << 20;
constexpr unsigned WS_BYTES = 26u << 20;

constexpr unsigned L_STAGE = 24576;
constexpr unsigned L_AUX = 98304;
constexpr unsigned L_LN = 122880;
constexpr unsigned L_WM = 131840;
constexpr unsigned L_HK = 148224;
constexpr unsigned L_TAB = 155648;
constexpr unsigned L_MOD = L_TAB;
constexpr unsigned L_V = L_TAB + 1280;
constexpr unsigned L_ST = L_TAB + 3328;
constexpr unsigned L_SY = L_TAB + 4352;
constexpr unsigned LDS_BYTES = 163840;
enum { SY_ADONE = 0, SY_STAT = 4, SY_UFIN = 5, SY_GSIG = 9, SY_WL = 10, SY_PROG = 14, SY_LN = 16, SY_ND = 17, SY_AUX = 18, SY_PQL = 19, SY_WD = 20, SY_X3 = 24, SY_X5 = 28, SY_WIS = 32, SY_K3D = 33, SY_N = 34, SY_EP = 35, SY_BAP = 36 };
constexpr unsigned L_RS = L_TAB + 6400;
#define PRE_H 4
#define L_PACE 6
#define L_EARLY 26
#define G_WIN 4
#define W_SIG 21
#define H1 6
#define H2 16
#define PQ_SIG 40
#define W_PACE 4
#define WD_PACE 6

constexpr int SPIN_CAP = 1 << 20;
constexpr int CODE_LINES_PER_WG = 10;

struct Params {
  const u16 *x, *wsre, *wsim, *wdre, *wdim, *wg, *wu, *wdn, *c, *wmod;
  u16* out;
  char* ws;
};

#if defined(__HIP_DEVICE_COMPILE__)
#pragma clang attribute push(__attribute__((target("no-packed-fp32-ops"))), apply_to = function)
#endif
DI float u2f(unsigned u) { return __builtin_bit_cast(float, u); }
DI unsigned f2u(float f) { return __builtin_bit_cast(unsigned, f); }
DI bool all_lanes(bool c) { return __builtin_amdgcn_ballot_w64(c) == ~0ull; }
DI u32x4 mk_srd(const void* p, unsigned bytes) {
  u64 a = (u64)p;
  u32x4 r = {(unsigned)a, (unsigned)(a >> 32) & 0xffffu, bytes, 0x00020000u};
  return r;
}
DI __amdgpu_buffer_rsrc_t mk_rsrc(const void* p, unsigned bytes) {
  return __builtin_amdgcn_make_buffer_rsrc((void*)p, 0, bytes, 0x00020000);
}
#define RFL(x) __builtin_amdgcn_readfirstlane(x)
#define BAR() __builtin_amdgcn_s_barrier()
#define WAITV(n) asm volatile("s_waitcnt vmcnt(%0)" ::"i"(n) : "memory")
#define WAITL0() __builtin_amdgcn_s_waitcnt(0xC07F)
#define CFENCE() asm volatile("" ::: "memory")

DI void dma4v(u32x4 srd, unsigned v0, unsigned v1, unsigned v2, unsigned v3, unsigned soff, unsigned m0v) {
  asm volatile("s_mov_b32 m0, %0\n\ts_nop 0\n\t"
               "buffer_load_dwordx4 %1, %5, %6 offen lds\n\t"
               "buffer_load_dwordx4 %2, %5, %6 offen offset:1024 lds\n\t"
               "buffer_load_dwordx4 %3, %5, %6 offen offset:2048 lds\n\t"
               "buffer_load_dwordx4 %4, %5, %6 offen offset:3072 lds"
               ::"s"(m0v), "v"(v0), "v"(v1), "v"(v2), "v"(v3), "s"(srd), "s"(soff) : "memory");
}
DI void dma2v(u32x4 srd, unsigned v0, unsigned v1, unsigned soff, unsigned m0v) {
  asm volatile("s_mov_b32 m0, %0\n\ts_nop 0\n\t"
               "buffer_load_dwordx4 %1, %3, %4 offen lds\n\t"
               "buffer_load_dwordx4 %2, %3, %4 offen offset:1024 lds"
               ::"s"(m0v), "v"(v0), "v"(v1), "s"(srd), "s"(soff) : "memory");
}
DI void dma2n(u32x4 srd, unsigned v0, unsigned v1, unsigned soff, unsigned m0v) {
  asm volatile("s_mov_b32 m0, %0\n\ts_nop 0\n\t"
               "buffer_load_dwordx4 %1, %3, %4 offen nt lds\n\t"
               "buffer_load_dwordx4 %2, %3, %4 offen offset:1024 nt lds"
               ::"s"(m0v), "v"(v0), "v"(v1), "s"(srd), "s"(soff) : "memory");
}
DI void dma2c(u32x4 srd, unsigned v, unsigned soff, unsigned m0v) {
  asm volatile("s_mov_b32 m0, %0\n\ts_nop 0\n\t"
               "buffer_load_dwordx4 %1, %2, %3 offen sc1 lds\n\t"
               "buffer_load_dwordx4 %1, %2, %3 offen offset:1024 sc1 lds"
               ::"s"(m0v), "v"(v), "s"(srd), "s"(soff) : "memory");
}
DI void dma4c(u32x4 srd, unsigned v, unsigned soff, unsigned m0v) {
  asm volatile("s_mov_b32 m0, %0\n\ts_nop 0\n\t"
               "buffer_load_dwordx4 %1, %2, %3 offen sc1 lds\n\t"
               "buffer_load_dwordx4 %1, %2, %3 offen offset:1024 sc1 lds\n\t"
               "buffer_load_dwordx4 %1, %2, %3 offen offset:2048 sc1 lds\n\t"
               "buffer_load_dwordx4 %1, %2, %3 offen offset:3072 sc1 lds"
               ::"s"(m0v), "v"(v), "s"(srd), "s"(soff) : "memory");
}
DI void dma1w(u32x4 srd, unsigned v, unsigned soff, unsigned m0v) {
  asm volatile("s_mov_b32 m0, %0\n\ts_nop 0\n\tbuffer_load_dword %1, %2, %3 offen sc1 lds" ::"s"(m0v), "v"(v), "s"(srd), "s"(soff) : "memory");
}
DI void dma1c(u32x4 srd, unsigned v, unsigned soff, unsigned m0v) {
  asm volatile("s_mov_b32 m0, %0\n\ts_nop 0\n\tbuffer_load_dwordx4 %1, %2, %3 offen sc1 lds" ::"s"(m0v), "v"(v), "s"(srd), "s"(soff) : "memory");
}
DI unsigned opv(unsigned v) { asm volatile("" : "+v"(v)); return v; }
DI int ops(int v) { asm volatile("" : "+s"(v)); return v; }
#define SCHED() __builtin_amdgcn_sched_barrier(0)
#define SGB(m, n) __builtin_amdgcn_sched_group_barrier((m), (n), 0)
DI bf16x8 lds_frag(unsigned a) { return *(const LAS bf16x8*)a; }
DI f32x4 lds_f4(unsigned a) { return *(const LAS f32x4*)a; }
DI bf16x8 lds_tr2(unsigned a0, unsigned a1) {
  s16x4 lo = __builtin_amdgcn_ds_read_tr16_b64_v4i16((LAS s16x4*)a0);
  s16x4 hi = __builtin_amdgcn_ds_read_tr16_b64_v4i16((LAS s16x4*)a1);
  return __builtin_shufflevector(lo, hi, 0, 1, 2, 3, 4, 5, 6, 7);
}
DI unsigned pk2(float a, float b) { bf2 p = {(__bf16)a, (__bf16)b}; return __builtin_bit_cast(unsigned, p); }
DI float bflo(unsigned u) { return u2f(u << 16); }
DI float bfhi(unsigned u) { return u2f(u & 0xffff0000u); }
DI float rbf(float a) { return (float)(__bf16)a; }
DI float sum32(float v) { auto r = __builtin_amdgcn_permlane32_swap(f2u(v), f2u(v), false, false); return u2f(r[0]) + u2f(r[1]); }
DI float sum16(float v) { auto r = __builtin_amdgcn_permlane16_swap(f2u(v), f2u(v), false, false); return u2f(r[0]) + u2f(r[1]); }
template <int CTRL> DI float dpp_add(float v) { return v + u2f(__builtin_amdgcn_update_dpp(0u, f2u(v), CTRL, 0xf, 0xf, true)); }
DI float wave_sum(float v) {
  v = dpp_add<0xB1>(v);
  v = dpp_add<0x4E>(v);
  v = dpp_add<0x141>(v);
  v = dpp_add<0x140>(v);
  return sum32(sum16(v));
}
#define MFMA16(a, b, c) __builtin_amdgcn_mfma_f32_16x16x32_bf16((a), (b), (c), 0, 0, 0)

DI unsigned atomic_peek(const char* p) {
  unsigned r, z = 0;
  asm volatile("global_atomic_add %0, %1, %2, off sc0\n\ts_waitcnt vmcnt(0)" : "=&v"(r) : "v"(p), "v"(z) : "memory");
  return r;
}
DI void cnt_add(char* p) { asm volatile("global_atomic_add %0, %1, off" ::"v"(p), "v"(1u) : "memory"); }
DI unsigned flag_ld(const char* p) { return __hip_atomic_load((GAS unsigned*)p, __ATOMIC_RELAXED, __HIP_MEMORY_SCOPE_AGENT); }
DI void flag_st(char* p, unsigned v) { asm volatile("global_atomic_swap %0, %1, off" ::"v"(p), "v"(v) : "memory"); }
DI void round_set(char* ws, int round, int b, unsigned lane, unsigned epoch) {
  if (lane < 16) flag_st(ws + WS_FLAG + round * 16384 + lane * 1024 + (((b >> 1) & 3) * 64 + (b >> 3) * 2 + (b & 1)) * 4, epoch);
}
DI const char* round_word(const char* ws, int round, int b, int quarter, unsigned lane) { return ws + WS_FLAG + round * 16384 + ((b >> 3) & 15) * 1024 + (quarter * 64 + lane) * 4; }
DI void cnt_wait(const char* p, unsigned base, int lane, int nl) {
_Pragma("unroll 1") for (int sp = 0; sp < SPIN_CAP; ++sp) {
    unsigned v = base + 16u;
    if (lane < nl) v = atomic_peek(p);
    if (all_lanes(v - base >= 16u)) break;
  }
  CFENCE();
}
DI void lds_wait_ge(unsigned word, unsigned want) {
  volatile LAS unsigned* p = (volatile LAS unsigned*)(L_SY + word * 4);
_Pragma("unroll 1") for (int sp = 0; sp < SPIN_CAP; ++sp) {
    if (RFL(*p) >= want) break;
    __builtin_amdgcn_s_sleep(1);
  }
  CFENCE();
}
DI void lds_wait4(unsigned word) {
  volatile LAS u32x4* p = (volatile LAS u32x4*)(L_SY + word * 4);
_Pragma("unroll 1") for (int sp = 0; sp < SPIN_CAP; ++sp) {
    const u32x4 v = *p;
    if (RFL(v[0] & v[1] & v[2] & v[3])) break;
    __builtin_amdgcn_s_sleep(1);
  }
  CFENCE();
}
DI unsigned lds_get(unsigned word) { return *(volatile LAS unsigned*)(L_SY + word * 4); }
DI void lds_set(unsigned word, unsigned v) { *(volatile LAS unsigned*)(L_SY + word * 4) = v; }
DI unsigned lds_inc(unsigned word) { return __hip_atomic_fetch_add((LAS unsigned*)(L_SY + word * 4), 1u, __ATOMIC_RELAXED, __HIP_MEMORY_SCOPE_WORKGROUP); }

#define STAMP(i) do { if ((i) == PROBE && threadIdx.x < 64) probe_t = (unsigned)__builtin_amdgcn_s_memrealtime(); } while (0)
#define STAMPB(i)

template <int I> struct IC { static constexpr int value = I; };
template <int A, int B, class F> DI void static_for(F&& f) { if constexpr (A < B) { f(IC<A>{}); static_for<A + 1, B>(f); } }

DI u32x4 merge16(unsigned a0, unsigned a1, unsigned b0, unsigned b1) {
  auto s0 = __builtin_amdgcn_permlane16_swap(a0, b0, false, false);
  auto s1 = __builtin_amdgcn_permlane16_swap(a1, b1, false, false);
  const u32x4 o = {s0[0], s1[0], s0[1], s1[1]};
  return o;
}

DI void pair_reduce(f32x4 (&y)[4], const f32x4 (&y2)[2][4], unsigned scratch, unsigned word, int w, unsigned lane) {
  const bool odd = w & 1;
  const unsigned a = scratch + lane * 16;
#pragma unroll
  for (int e = 0; e < 4; ++e) *(LAS f32x4*)(a + w * 4096 + e * 1024) = odd ? y2[0][e] : y2[1][e];
  CFENCE();
  lds_set(word + w, 1);
  lds_wait_ge(word + (w ^ 1), 1);
#pragma unroll
  for (int e = 0; e < 4; ++e) y[e] = (odd ? y2[1][e] : y2[0][e]) + lds_f4(a + (w ^ 1) * 4096 + e * 1024);
}

DI void load_w(bf16x8 (&W)[32], __amdgpu_buffer_rsrc_t rws, int c, int rot, unsigned lane) {
  const unsigned vl = opv(lane * 16);
#pragma unroll
  for (int k = 0; k < 32; ++k) {
    W[k] = __builtin_bit_cast(bf16x8, __builtin_amdgcn_raw_buffer_load_b128(rws, vl + (k & 3) * 1024, WS_WT + (unsigned)(c * 32 + (((k >> 2) + rot) & 7) * 4) * 1024u, 16));
    if ((k & 1) == 1 && k >= W_PACE && k < 31) WAITV(W_PACE);
    if (k == W_SIG && lane == 0) lds_inc(SY_WIS);
  }
  SCHED();
}
template <int N> DI float row_bcast(float v) { return u2f(__builtin_amdgcn_update_dpp(0u, f2u(v), 0x150 + N, 0xf, 0xf, true)); }

DI float geglu(float g, float u, float rstd, float nmr, float ug, float vg, float uu, float vu) {
  const float a = fmaf(g, rstd, fmaf(nmr, ug, vg));
  const float b = fmaf(u, rstd, fmaf(nmr, uu, vu));
  const float e = a * fmaf(a * a, -0.10294324f, -2.30220820f);
  return a * b * __builtin_amdgcn_rcpf(1.f + __builtin_amdgcn_exp2f(e));
}

DI void k4_body(const bf16x8 (&W)[32], __amdgpu_buffer_rsrc_t rws, char* ws, int wv, unsigned lane, int tp4, int ft, int rot) {
  const unsigned q = lane >> 4, r16 = lane & 15;
  const int c = 7 * ft + wv;
  f32x4 ug, vg, uu, vu;
  {
    f32x4 dg = {}, du = {};
    const unsigned va = L_V + (lane & 1) * 1024 + q * 16;
#pragma unroll
    for (int k = 0; k < 16; ++k) {
      const bf16x8 f = lds_frag(va + (unsigned)((((k >> 1) + rot) & 7) * 128 + (k & 1) * 64));
      dg = MFMA16(W[2 * k], f, dg); du = MFMA16(W[2 * k + 1], f, du);
    }
#pragma unroll
    for (int j = 0; j < 4; ++j) { vg[j] = row_bcast<0>(dg[j]); ug[j] = row_bcast<1>(dg[j]); vu[j] = row_bcast<0>(du[j]); uu[j] = row_bcast<1>(du[j]); }
  }
  SCHED();
  const unsigned gvoff = (q & 1) * 57344 + (c >> 1) * 1024 + (c & 1) * 512 + (q >> 1) * 256 + r16 * 16;
  const unsigned sb = opv(L_ST + r16 * 8);
  unsigned ab[8];
#pragma unroll
  for (int g = 0; g < 8; ++g) ab[g] = lane * 16 + (unsigned)(((g + rot) & 7) * 2048);
  lds_wait_ge(SY_ADONE + 0, 1);
#pragma unroll 1
  for (int u = 0; u < 4; ++u) {
    const int tbl = (u + ft) & 3;

    f32x4 gx = {}, ux = {}, gy = {}, uy = {};
    bf16x8 A[32];
    auto rda = [&](int i) { A[i] = lds_frag(ab[i >> 2] + ((i >> 1) & 1) * 1024 + (i & 1) * 16384); };
    static_for<0, 6>([&](auto ic) { rda(decltype(ic)::value); });
    unsigned nxt, sto;
    SCHED();
    static_for<0, 32>([&](auto ic) {
      constexpr int i = decltype(ic)::value;
      if constexpr (i & 1) { gy = MFMA16(W[i - 1], A[i], gy); SCHED(); uy = MFMA16(W[i], A[i], uy); }
      else { gx = MFMA16(W[i], A[i], gx); SCHED(); ux = MFMA16(W[i + 1], A[i], ux); }
      SCHED();
      if constexpr (i + 6 < 32) rda(i + 6);
      if constexpr (i == 26) nxt = lds_get(SY_ADONE + u + 1);
      if constexpr (i == 27) sto = lds_get(SY_STAT);
      SCHED();
    });

    if (lane == 0) lds_inc(SY_UFIN + u);
    if (!RFL(sto)) lds_wait_ge(SY_STAT, 1);
    const f32x2 sx = *(const LAS f32x2*)(sb + tbl * 256), sy = *(const LAS f32x2*)(sb + tbl * 256 + 128);
#pragma unroll
    for (int g = 0; g < 8; ++g) ab[g] += 32768;
    float ox[4], oy[4];
#pragma unroll
    for (int j = 0; j < 4; ++j) {
      ox[j] = geglu(gx[j], ux[j], sx[0], sx[1], ug[j], vg[j], uu[j], vu[j]);
      oy[j] = geglu(gy[j], uy[j], sy[0], sy[1], ug[j], vg[j], uu[j], vu[j]);
    }
    __builtin_amdgcn_raw_buffer_store_b128(merge16(pk2(ox[0], ox[1]), pk2(ox[2], ox[3]), pk2(oy[0], oy[1]), pk2(oy[2], oy[3])), rws, gvoff,
                                           WS_G + (unsigned)(2 * (4 * tp4 + tbl)) * 57344u, 16);
    if (!RFL(nxt)) lds_wait_ge(SY_ADONE + u + 1, 1);
  }
  WAITV(0);
  unsigned old = 0;
  if (lane == 0) old = lds_inc(SY_GSIG);
  if (RFL(old) == 6 && lane < 4) cnt_add(ws + WS_CG + (4 * tp4 + lane) * 256);
}

#define C_XCD(b) ((b) & 7)
#define C_LOC(b) ((b) >> 3)
#define C_TP(b) (2 * C_XCD(b) + (C_LOC(b) >> 4))
#define C_PART(b) ((C_LOC(b) >> 3) & 1)
#define C_DN(b) (C_LOC(b) & 7)
#define C_J0(b) (4 * ((C_DN(b) + C_XCD(b) + 4 * C_PART(b)) & 7))
#define C_TB64(b) (4 * C_XCD(b) + (C_LOC(b) >> 3))
#define C_TP4(b) (8 * (C_XCD(b) & 1) + (C_LOC(b) >> 2))
#define C_FT(b) (4 * (C_XCD(b) >> 1) + (C_LOC(b) & 3))
#define C_ROT5(b) ((7 * C_DN(b) / 4 + 7 * (C_TB64(b) & 3) / 2) % 14)

__global__ __launch_bounds__(512, 2) void fused_block(Params p) {
  const int b0 = blockIdx.x;
  const unsigned lane0 = threadIdx.x & 63;
  const int wv = RFL(threadIdx.x >> 6);
  char* const ws = p.ws;
  u64 pc0;
  asm volatile("s_getpc_b64 %0" : "=s"(pc0));
  unsigned probe_t = 0;
  const unsigned probe_0 = (unsigned)__builtin_amdgcn_s_memrealtime();
  STAMP(0);
  const u32x4 sws = mk_srd(ws, WS_BYTES);
  const __amdgpu_buffer_rsrc_t rws = mk_rsrc(ws, WS_BYTES);

  if (wv >= 4) {
    const int wl = wv - 4;
    const unsigned lane = lane0;
    const unsigned sw = (lane & 7) ^ (lane >> 4);
    const unsigned vl = lane * 16;
    {
      const int b = b0, j0 = C_J0(b), dn = C_DN(b);
      unsigned junk = 0;
      if (wv == 4 && lane < CODE_LINES_PER_WG)
        asm volatile("global_load_dword %0, %1, off" : "=&v"(junk) : "v"((const char*)(pc0 & ~255ull) + (C_LOC(b) * CODE_LINES_PER_WG + lane) * 128) : "memory");
      const u32x4 sA = mk_srd(C_PART(b) ? p.wsim : p.wsre, 2048u * 2048u * 2u);
      const unsigned ra = (unsigned)(128 * C_TP(b) + 32 * wl + (lane >> 3)) * 4096u;
      const unsigned va0 = ra + sw * 16, va1 = ra + 8 * 4096 + (sw ^ 4) * 16 - 1024, va2 = ra + 16 * 4096 + sw * 16 - 2048, va3 = ra + 24 * 4096 + (sw ^ 4) * 16 - 3072;
      const unsigned o1 = (lane & 11) == 0 ? 0x3f803f80u : 0u;
      const u32x4 ones4 = {o1, o1, o1, o1};
      const bf16x8 sel = __builtin_bit_cast(bf16x8, ones4);
      const unsigned wa = (32 * wl + (lane & 15)) * 128 + (((lane >> 4) ^ ((lane >> 1) & 7)) * 16);
      f32x4 r0 = {}, r1 = {};
      auto rsum = [&](int s) {
        const unsigned a = wa + s * L_STAGE;
        r0 = MFMA16(sel, lds_frag(a), r0); r1 = MFMA16(sel, lds_frag(a + 2048), r1);
        r0 = MFMA16(sel, lds_frag(a ^ 64), r0); r1 = MFMA16(sel, lds_frag((a ^ 64) + 2048), r1);
      };
      auto half = [&](int hs) {
        if (hs & 1) dma2n(sA, va2 + 2048, va3 + 2048, (unsigned)(j0 + (hs >> 1)) * 128u, (hs >> 1) * L_STAGE + wl * 4096 + 2048);
        else dma2n(sA, va0, va1, (unsigned)(j0 + (hs >> 1)) * 128u, (hs >> 1) * L_STAGE + wl * 4096);
      };
      half(0);
      BAR();
#pragma unroll
      for (int hs = 1; hs < PRE_H; ++hs) half(hs);
      STAMP(1);
      BAR();
      asm volatile("" : "+v"(junk));
#pragma unroll
      for (int hs = PRE_H; hs < 8; ++hs) half(hs);
      asm volatile("s_waitcnt vmcnt(12)" : "+v"(junk)::"memory");
      BAR();
      STAMP(2);
      rsum(0);
      WAITV(8); BAR();
      STAMPB(1);
      auto stage1 = [&](int s) {
        const unsigned j = (unsigned)(j0 + s) & 31u, sl = (unsigned)(s & 3) * L_STAGE;
        WAITV(L_PACE);
        dma2c(sws, vl, WS_NT + ((unsigned)dn * 32u + j) * 8192u + wl * 2048, sl + 16384 + wl * 2048);
      };
      auto stage2 = [&](int s) {
        const unsigned j = (unsigned)(j0 + s) & 31u, sl = (unsigned)(s & 3) * L_STAGE;
        WAITV(L_PACE);
        dma2v(sA, va0, va1, j * 128u, sl + wl * 4096);
        WAITV(L_PACE);
        dma2v(sA, va2 + 2048, va3 + 2048, j * 128u, sl + wl * 4096 + 2048);
      };
      auto stage = [&](int s) { stage1(s); stage2(s); };
      rsum(1);
      WAITL0();
      stage(4);
      WAITV(10); BAR();
      STAMPB(2);
      rsum(2);
      stage(5);
      WAITV(12); BAR();
      STAMPB(3);
      rsum(3);
      if (lane < 32) __builtin_amdgcn_raw_buffer_store_b32(f2u(lane < 16 ? r0[0] : r1[0]), rws, lane * 4, WS_RSP + (unsigned)((dn * 2 + C_PART(b)) * 2048 + 128 * C_TP(b) + 32 * wl) * 4u, 16);
      stage(6);
      WAITV(12); BAR();
      STAMPB(4);
#pragma unroll 1
      for (int s = 5; s < L_EARLY; ++s) { stage(s + 2); WAITV(12); BAR(); STAMPB(s); if ((s & 3) == 0) STAMP(23 + (s >> 2)); }
#pragma unroll 1
      for (int s = L_EARLY; s <= 29; ++s) { stage1(s + 2); WAITV(8); BAR(); STAMPB(s); if ((s & 3) == 0) STAMP(23 + (s >> 2)); stage2(s + 2); }
    }
    const int b = ops(b0);
    {
      auto wdblk = [&](int n) {
        const unsigned blk = (unsigned)(n + 4 * (C_TB64(b) & 3)) & 15u;
        dma2c(sws, vl, WS_WDH + ((unsigned)C_DN(b) * 16u + blk) * 8192u + wl * 2048, (unsigned)(n < 7 ? 12 + n : n - 7) * 8192u + wl * 2048);
      };
      WAITV(6); BAR();
      STAMPB(30);
      WAITV(4); wdblk(0); WAITV(4); wdblk(1);
      WAITV(4); BAR();
      STAMP(3);
#pragma unroll 1
      for (int n = 2; n < 16; ++n) { WAITV(WD_PACE); wdblk(n); if ((n & 3) == 0) STAMP(9 + (n >> 2)); }
    }
    const int tp4 = C_TP4(b), ft = C_FT(b), rot4 = C_LOC(b) >> 2;
    if (wv < 7) {
      bf16x8 W[32];
      WAITV(0);
      lds_set(SY_WD + wl, 1);
      STAMP(4);
      lds_wait_ge(SY_AUX, 1);
      lds_wait_ge(SY_PQL, 4);
      load_w(W, rws, 7 * ft + wv, rot4, lane);
      STAMP(5);
      k4_body(W, rws, ws, wv, lane, tp4, ft, rot4);
      STAMP(8);
    } else {
      WAITV(0);
      lds_set(SY_WD + 3, 1);
      STAMP(4);
      {
        const unsigned ep = RFL(lds_get(SY_EP));
        const char* fp = round_word(ws, 2, b, 0, lane);
_Pragma("unroll 1") for (int sp = 0; sp < SPIN_CAP; ++sp) {
          unsigned f0, f1, f2, f3;
          asm volatile("global_atomic_add %0, %4, %5, off sc0\n\tglobal_atomic_add %1, %4, %5, off offset:256 sc0\n\tglobal_atomic_add %2, %4, %5, off offset:512 sc0\n\t"
                       "global_atomic_add %3, %4, %5, off offset:768 sc0\n\ts_waitcnt vmcnt(0)"
                       : "=&v"(f0), "=&v"(f1), "=&v"(f2), "=&v"(f3) : "v"(fp), "v"(0u) : "memory");
          if (all_lanes(f0 == ep && f1 == ep && f2 == ep && f3 == ep)) break;
          __builtin_amdgcn_s_sleep(12);
        }
        lds_set(SY_AUX, 1);
        STAMP(9);
      }
      const unsigned baseAP = lds_get(SY_BAP + (lane & 3));
      lds_wait_ge(SY_K3D, 4);
      STAMP(5);
      const int rot5 = C_ROT5(b), et = C_DN(b);
      auto wdn = [&](int n0, int n1) {
#pragma unroll 1
        for (int n = n0; n < n1; ++n) {
          int kb = (n >> 1) + rot5; kb = (kb >= 14 ? kb - 14 : kb) + 14 * (n & 1);
          int sn = n + 16; sn = sn >= 38 ? sn - 38 : sn >= 19 ? sn - 19 : sn;
          const unsigned so = WS_WDT + (unsigned)(et * 28 + kb) * 8192u, sl = (unsigned)sn * 8192u;
          dma4c(sws, vl, so, sl); dma4c(sws, vl, so + 4096, sl + 4096);
          WAITV(8);
        }
      };
      lds_wait_ge(SY_WIS, 7);
      STAMP(13);
      cnt_wait(ws + WS_CAP + (4 * tp4 + lane) * 256, baseAP, lane, 4);
      STAMP(6);
      auto ablk = [&](int h0, int h1) {
#pragma unroll 1
        for (int hh = h0; hh < h1; ++hh) {
          const unsigned so = WS_AP + (unsigned)(4 * tp4 + (((hh >> 1) + ft) & 3)) * 32768u + (hh & 1) * 16384, sl = (unsigned)hh * 16384u;
#pragma unroll
          for (int q = 0; q < 4; ++q) dma4c(sws, vl, so + q * 4096, sl + q * 4096);
        }
      };
      ablk(0, 3);
      WAITV(16); lds_set(SY_ADONE + 0, 1);
      STAMP(7);
      ablk(3, 4);
      u32x2 sp[2][8];
#pragma unroll
      for (int hf = 0; hf < 2; ++hf)
#pragma unroll
        for (int e = 0; e < 8; ++e)
          sp[hf][e] = __builtin_amdgcn_raw_buffer_load_b64(rws, lane * 8 + hf * 512, WS_STATS + (unsigned)(e * 2048 + 128 * tp4) * 8u, 16);
      CFENCE();
      WAITV(16); lds_set(SY_ADONE + 1, 1);
      STAMP(16);
#pragma unroll
      for (int hf = 0; hf < 2; ++hf) {
        float mu = 0.f, m2 = 0.f;
#pragma unroll
        for (int e = 0; e < 8; ++e) { mu += u2f(sp[hf][e][0]); m2 += u2f(sp[hf][e][1]); }
        mu *= 0.125f;
        float dv = 0.f;
#pragma unroll
        for (int e = 0; e < 8; ++e) { const float d = u2f(sp[hf][e][0]) - mu; dv = fmaf(d, d, dv); }
        const float rstd = __builtin_amdgcn_rsqf(fmaf(dv, 64.f, m2) * (1.f / 512.f) + 1e-5f);
        const f32x2 o = {rstd, -mu * rstd};
        *(LAS f32x2*)(L_ST + (64 * hf + lane) * 8) = o;
      }
      WAITL0(); lds_set(SY_STAT, 1);
      STAMP(17);
      ablk(4, 7);
      WAITV(16); lds_set(SY_ADONE + 2, 1);
      STAMP(18);
      ablk(7, 8);
      WAITV(0); lds_set(SY_ADONE + 3, 1);
      STAMP(19);
      wdn(0, 3);
      lds_wait_ge(SY_UFIN + 0, 7); wdn(3, 7);
      lds_wait_ge(SY_UFIN + 1, 7); wdn(7, 11);
      lds_set(SY_WL + 0, 1);
      STAMP(20);
      lds_wait_ge(SY_UFIN + 2, 7); wdn(11, 15);
      lds_wait_ge(SY_UFIN + 3, 7); wdn(15, 19);
      WAITV(0); lds_set(SY_WL + 1, 1);
      STAMP(8);
      lds_wait_ge(SY_PROG + 0, 4);
      wdn(19, 27);
      lds_set(SY_WL + 2, 1);
      STAMP(21);
      lds_wait_ge(SY_PROG + 1, 4);
      wdn(27, 28);
      WAITV(0); lds_set(SY_WL + 3, 1);
      STAMP(22);
    }
  } else {
    const int w = wv;
    unsigned epoch, basePQ, baseG;
    {
      const int b = b0;
      const unsigned lane = lane0, q = lane >> 4, r16 = lane & 15;
      u32x4 xa, xb;
      asm volatile("global_load_dwordx4 %0, %2, off nt\n\tglobal_load_dwordx4 %1, %2, off offset:1024 nt" : "=&v"(xa), "=&v"(xb) : "v"((const char*)p.x + (8 * b + 2 * w) * 1024 + lane * 16) : "memory");
      unsigned sv = 0, ev;
      STAMP(28);
      asm volatile("global_load_dword %0, %1, off sc1" : "=&v"(ev) : "v"(ws + WS_NONCE) : "memory");
      {
        const unsigned off = lane < 2 ? (lane ? WS_CG : WS_CPQ) + (2 * C_TB64(b) + (w >> 1)) * 256 : WS_CAP + (4 * C_TP4(b) + lane - 2) * 256;
        if (lane < (w == 0 ? 6u : 2u)) asm volatile("global_atomic_add %0, %1, %2, off sc0" : "=&v"(sv) : "v"(ws + off), "v"(0u) : "memory");
      }
      if (threadIdx.x < SY_N) lds_set(threadIdx.x, 0);
      WAITL0(); BAR();
      __builtin_amdgcn_s_setprio(1);
      asm volatile("s_waitcnt vmcnt(2)" : "+v"(xa), "+v"(xb)::"memory");
      STAMP(13);
      {
        const bf2 one = __builtin_bit_cast(bf2, 0x3f803f80u);
        float sa = 0.f, sb = 0.f, qa = 0.f, qb = 0.f;
#pragma unroll
        for (int i = 0; i < 4; ++i) {
          const unsigned ua = xa[i], ub = xb[i];
          const bf2 pa = __builtin_bit_cast(bf2, ua), pb = __builtin_bit_cast(bf2, ub);
          sa = __builtin_amdgcn_fdot2_f32_bf16(pa, one, sa, false); qa = __builtin_amdgcn_fdot2_f32_bf16(pa, pa, qa, false);
          sb = __builtin_amdgcn_fdot2_f32_bf16(pb, one, sb, false); qb = __builtin_amdgcn_fdot2_f32_bf16(pb, pb, qb, false);
        }
        sa = wave_sum(sa) * (1.f / 512.f); sb = wave_sum(sb) * (1.f / 512.f);
        qa = wave_sum(qa) * (1.f / 512.f); qb = wave_sum(qb) * (1.f / 512.f);
        qa = __builtin_amdgcn_rsqf(__builtin_fmaxf(fmaf(-sa, sa, qa), 0.f) + 1e-5f); qb = __builtin_amdgcn_rsqf(__builtin_fmaxf(fmaf(-sb, sb, qb), 0.f) + 1e-5f);
        sa *= -qa; sb *= -qb;
        u32x4 na, nb;
#pragma unroll
        for (int i = 0; i < 4; ++i) {
          na[i] = pk2(fmaf(bflo(xa[i]), qa, sa), fmaf(bfhi(xa[i]), qa, sa));
          nb[i] = pk2(fmaf(bflo(xb[i]), qb, sb), fmaf(bfhi(xb[i]), qb, sb));
        }
        *(LAS u32x4*)(L_LN + (2 * w) * 1088 + lane * 16) = na;
        *(LAS u32x4*)(L_LN + (2 * w + 1) * 1088 + lane * 16) = nb;
      }
      CFENCE();
      if (lane == 0) lds_inc(SY_LN);
      lds_wait_ge(SY_LN, 4);
      {
        const unsigned la = L_LN + (r16 >> 2) * 1088 + (128 * w + 16 * q + 4 * (lane & 3)) * 2;
#pragma unroll
        for (int i = 0; i < 2; ++i) {
          const bf16x8 f = lds_tr2(la + i * 128, la + i * 128 + 4 * 1088);
          __builtin_amdgcn_raw_buffer_store_b128(__builtin_bit_cast(u32x4, f), rws, lane * 16, WS_NT + (unsigned)((2 * w + i) * 256 + b) * 1024u, 16);
        }
      }
      STAMP(14);
      asm volatile("s_waitcnt vmcnt(0)" : "+v"(sv), "+v"(ev)::"memory");
      basePQ = __builtin_amdgcn_readlane(sv, 0); baseG = __builtin_amdgcn_readlane(sv, 1);
      epoch = RFL(ev) + 1u;
      unsigned old = 0;
      if (lane == 0) old = lds_inc(SY_ND);
      if (RFL(old) == 3) round_set(ws, 0, b, lane, epoch);
      STAMP(1);
      if (w == 0) { if (lane >= 2 && lane < 6) lds_set(SY_BAP + lane - 2, sv); if (lane == 6) lds_set(SY_EP, epoch); }
      {
        const char* fp = round_word(ws, 0, b, w, lane);
_Pragma("unroll 1") for (int sp = 0; sp < SPIN_CAP; ++sp)
          if (all_lanes(atomic_peek(fp) == epoch)) break;
      }
#pragma unroll
      for (int s = 0; s < 4; ++s) dma2c(sws, lane * 16, WS_NT + (unsigned)(C_DN(b) * 32 + C_J0(b) + s) * 8192u + w * 2048, s * L_STAGE + 16384 + w * 2048);
      BAR();
      STAMP(2);
      const unsigned m0 = (unsigned)(128 * (b & 3) + 32 * w + (lane >> 3)) * 6144u + (lane & 7) * 16 + (b < 192 ? (b >> 2) * 128 : 0x40000000);
      const u32x4 sM = mk_srd(p.wmod, 512u * 3072u * 2u);
      dma2n(sM, m0, m0 + 8 * 6144 - 1024, 0, L_WM + w * 4096);
      dma2n(sM, m0 + 16 * 6144, m0 + 24 * 6144 - 1024, 0, L_WM + w * 4096 + 2048);
      dma1w(mk_srd(p.c, 1024u), lane * 4, (b & 3) * 256, L_HK + w * 256);
      const unsigned h0 = (unsigned)(64 * (b & 7) + 16 * w + r16) * 1024u + q * 16 + (b >= 128 ? 0 : 0x40000000);
      lds_wait_ge(SY_ND, 4);
      dma2n(mk_srd(b >= 192 ? p.wdim : p.wdre, 512u * 512u * 2u), h0, h0 + 64 - 1024, ((b >> 3) & 7) * 128, L_LN + w * 2048);
    }

    u32x2 xr[4];
    {
      f32x4 acc[2][4] = {};
      bf16x8 N[2][8], Wf[2][4];
      const unsigned nb0 = (lane0 >> 4) * 1024 + (lane0 & 15) * 16 + 16384;
      const unsigned wa0 = (32 * w + (lane0 & 15)) * 128 + (((lane0 >> 4) ^ ((lane0 >> 1) & 7)) * 16);
      const unsigned nbA = opv(nb0), nbB = opv(nb0 + 2 * L_STAGE), waA = opv(wa0), waB = opv(wa0 + 2 * L_STAGE), wbA = opv(wa0 ^ 64), wbB = opv((wa0 ^ 64) + 2 * L_STAGE);
      auto rd = [&](int s) {
        const unsigned o = (s & 1) * L_STAGE;
        const unsigned nb = (s & 2) ? nbB : nbA, wa = (s & 2) ? waB : waA, wb = (s & 2) ? wbB : wbA;
        Wf[s & 1][0] = lds_frag(wa + o); Wf[s & 1][1] = lds_frag(wa + o + 2048);
#pragma unroll
        for (int i = 0; i < 4; ++i) N[s & 1][i] = lds_frag(nb + o + i * 256);
        Wf[s & 1][2] = lds_frag(wb + o); Wf[s & 1][3] = lds_frag(wb + o + 2048);
#pragma unroll
        for (int i = 0; i < 4; ++i) N[s & 1][4 + i] = lds_frag(nb + o + 4096 + i * 256);
      };
      auto mm = [&](int s) {
#pragma unroll
        for (int kk = 0; kk < 2; ++kk)
#pragma unroll
          for (int i = 0; i < 4; ++i) {
            acc[0][i] = MFMA16(N[s & 1][4 * kk + i], Wf[s & 1][2 * kk], acc[0][i]);
            acc[1][i] = MFMA16(N[s & 1][4 * kk + i], Wf[s & 1][2 * kk + 1], acc[1][i]);
          }
      };
      unsigned pk1 = 0;
      WAITV(13); BAR();
      STAMP(3);
      rd(0);
      WAITL0();
      WAITV(11);
      static_for<1, 32>([&](auto sc) {
        constexpr int s = decltype(sc)::value;
        SCHED(); BAR(); SCHED();
        rd(s);
        mm(s - 1);
#pragma unroll
        for (int i = 0; i < 12; ++i) { SGB(0x8, 1); SGB(0x100, 1); }
        SGB(0x8, 4);
        SCHED();
        WAITL0();
        SCHED();
        if constexpr (s == 1) {
          WAITV(9);
        } else if constexpr (s == 2) {
          WAITV(7);
        } else if constexpr (s == 3) {
          if (ops(b0) == 0 && w == 0 && lane0 == 0) flag_st(ws + WS_NONCE, epoch);
        } else if constexpr (s == H1) {
          const int b = ops(b0);
          const unsigned lane = opv(lane0);
          WAITV(0);
          LAS unsigned* cp = (LAS unsigned*)(L_HK + w * 256 + lane * 4);
          const unsigned cc = *cp;
          const bf16x8 f0 = lds_frag(L_LN + w * 2048 + lane * 16), f1 = lds_frag(L_LN + w * 2048 + 1024 + lane * 16);
          const float c0 = bflo(cc), c1 = bfhi(cc);
          *cp = pk2(c0 * __builtin_amdgcn_rcpf(1.f + __builtin_amdgcn_exp2f(c0 * -1.44269504f)), c1 * __builtin_amdgcn_rcpf(1.f + __builtin_amdgcn_exp2f(c1 * -1.44269504f)));
          if (b >= 128) {
            const unsigned so = WS_WDH + (unsigned)(((b & 7) * 16 + ((b >> 3) & 15)) * 8 + w) * 1024u;
            __builtin_amdgcn_raw_buffer_store_b128(__builtin_bit_cast(u32x4, f0), rws, lane * 16, so, 16);
            __builtin_amdgcn_raw_buffer_store_b128(__builtin_bit_cast(u32x4, f1), rws, lane * 16, so + 4096, 16);
          }
        } else if constexpr (s == H1 + 1) {
          const int b = ops(b0);
          const unsigned lane = opv(lane0), q = lane >> 4, r16 = lane & 15;
          f32x4 d = {}, d2 = {};
          const unsigned ma = L_WM + (8 * q + (r16 >> 2)) * 128 + (16 * w + 4 * (lane & 3)) * 2;
#pragma unroll
          for (int k = 0; k < 4; k += 2) {
            d = MFMA16(lds_frag(L_HK + w * 256 + (32 * k + 8 * q) * 2), lds_tr2(ma + k * 4096, ma + k * 4096 + 512), d);
            d2 = MFMA16(lds_frag(L_HK + w * 256 + (32 * k + 32 + 8 * q) * 2), lds_tr2(ma + k * 4096 + 4096, ma + k * 4096 + 4608), d2);
          }
          if (b < 192 && lane < 16) __builtin_amdgcn_raw_buffer_store_b32(f2u(d[0] + d2[0]), rws, lane * 4, WS_MODP + (unsigned)((b & 3) * 3072 + (b >> 2) * 64 + 16 * w) * 4u, 16);
          WAITL0();
        } else if constexpr (s == H1 + 4) {
          WAITV(0);
        } else if constexpr (s == H1 + 5) {
          if (w == 0) round_set(ws, 1, ops(b0), opv(lane0), epoch);
        } else if constexpr (s == H2) {
          const int b = ops(b0);
          const unsigned lane = opv(lane0);
          if (b < 224) {
            const unsigned g0 = (unsigned)(64 * (b / 28) + 16 * w + (lane >> 3)) * 3584u + (lane & 7) * 16;
            dma2n(mk_srd(p.wg, 512u * 1792u * 2u), g0, g0 + 8 * 3584 - 1024, (b % 28) * 128, L_AUX + w * 2048);
            dma2n(mk_srd(p.wu, 512u * 1792u * 2u), g0, g0 + 8 * 3584 - 1024, (b % 28) * 128, L_AUX + 8192 + w * 2048);
            const unsigned d0 = (unsigned)(64 * (b >> 3) + 16 * w + (lane >> 3)) * 1024u + (lane & 7) * 16;
            dma2n(mk_srd(p.wdn, 1792u * 512u * 2u), d0, d0 + 8 * 1024 - 1024, (b & 7) * 128, L_AUX + 16384 + w * 2048);
          }
          asm volatile("global_atomic_add %0, %1, %2, off sc0" : "=&v"(pk1) : "v"(round_word(ws, 1, b, w, lane)), "v"(0u) : "memory");
        } else if constexpr (s == H2 + 4) {
          asm volatile("s_waitcnt vmcnt(0)" : "+v"(pk1)::"memory");
          if (!all_lanes(pk1 == epoch)) {
            const char* fp = round_word(ws, 1, ops(b0), w, opv(lane0));
_Pragma("unroll 1") for (int sp = 0; sp < SPIN_CAP; ++sp) {
              if (all_lanes(atomic_peek(fp) == epoch)) break;
              __builtin_amdgcn_s_sleep(8);
            }
          }
          CFENCE();
        } else if constexpr (s == H2 + 5 || s == H2 + 6) {
          const int b = ops(b0);
          const unsigned lane = opv(lane0), q = lane >> 4, r16 = lane & 15;
          if constexpr (s == H2 + 5) {
            const int dn = C_DN(b);
            const int cb0 = (w == 3 ? 32 : 8 * w) + dn;
#pragma unroll
            for (int k = 0; k < 4; ++k) {
              dma1c(sws, lane * 16, WS_MODP + (unsigned)(k * 3072 + 1536 + 256 * w) * 4u, L_WM + w * 4096 + k * 1024);
              dma1w(sws, lane * 4, WS_MODP + (unsigned)(k * 3072 + cb0 * 64) * 4u, L_LN + w * 2048 + k * 256);
              dma1w(sws, (lane >> 5) * 16384 + (lane & 31) * 4, WS_RSP + (unsigned)((4 * k + C_PART(b)) * 2048 + 128 * C_TP(b) + 32 * w) * 4u, L_LN + w * 2048 + 1024 + k * 256);
            }
            dma1w(sws, lane * 4, WS_MODP + (unsigned)(w * 3072 + (40 + dn) * 64) * 4u, L_HK + w * 256);
          }
          if (b < 224) {
            const unsigned ga = L_AUX + (8 * q + (r16 >> 2)) * 128 + (16 * w + 4 * (lane & 3)) * 2;
#pragma unroll
            for (int k = 2 * (s - H2 - 5); k < 2 * (s - H2 - 5) + 2; ++k) {
              const unsigned a = ga + (k >> 1) * 4096 + (k & 1) * 8192;
              __builtin_amdgcn_raw_buffer_store_b128(__builtin_bit_cast(u32x4, lds_tr2(a, a + 512)), rws, lane * 16 + k * 1024, WS_WT + (unsigned)((4 * (b % 28) + w) * 32 + 4 * (b / 28)) * 1024u, 16);
            }
            const unsigned a = ga + 16384 + (s - H2 - 5) * 4096;
            __builtin_amdgcn_raw_buffer_store_b128(__builtin_bit_cast(u32x4, lds_tr2(a, a + 512)), rws, lane * 16, WS_WDT + (unsigned)(((b & 7) * 28 + (b >> 3)) * 8 + 4 * (s - H2 - 5) + w) * 1024u, 16);
          }
        } else if constexpr (s == H2 + 8) {
          const unsigned lane = opv(lane0);
          if (ops(b0) < 224) WAITV(6); else WAITV(0);
          const unsigned va = L_WM + w * 4096 + lane * 16;
          const f32x4 v = (lds_f4(va) + lds_f4(va + 1024)) + (lds_f4(va + 2048) + lds_f4(va + 3072));
          const LAS float* mp = (const LAS float*)(L_LN + w * 2048 + lane * 4);
          float m0 = rbf((mp[0] + mp[64]) + (mp[128] + mp[192]));
          const float rs = sum32((mp[256] + mp[320]) + (mp[384] + mp[448]));
          const float one = w >= 2 ? 1.f : 0.f;
          const u32x2 vb = {pk2(rbf(v[0]) + one, rbf(v[1]) + one), pk2(rbf(v[2]) + one, rbf(v[3]) + one)};
          *(LAS u32x2*)(L_V + w * 512 + lane * 8) = vb;
          if (w & 1) m0 = rbf(1.f + m0);
          if (w < 2 && C_PART(ops(b0))) m0 = -m0;
          *(LAS float*)(L_MOD + w * 256 + lane * 4) = m0;
          if (lane < 32) *(LAS float*)(L_RS + (32 * w + lane) * 4) = rs;
          WAITL0();
        } else if constexpr (s == H2 + 9) {
          if (w == 0) {
            const LAS float* mp = (const LAS float*)(L_HK + opv(lane0) * 4);
            *(LAS float*)(L_MOD + 1024 + opv(lane0) * 4) = rbf((mp[0] + mp[64]) + (mp[128] + mp[192]));
            WAITL0();
          }
        } else if constexpr (s == H2 + 10) {
          WAITV(0);
        } else if constexpr (s == H2 + 11) {
          if (w == 0) round_set(ws, 2, ops(b0), opv(lane0), epoch);
        } else if constexpr (s == 31) {
          const int b = ops(b0);
          const unsigned lane = opv(lane0);
          const __amdgpu_buffer_rsrc_t rx = mk_rsrc(p.x, 2048u * 512u * 2u);
#pragma unroll
          for (int e = 0; e < 4; ++e)
            xr[e] = __builtin_amdgcn_raw_buffer_load_b64(rx, (lane & 15) * 1024 + (lane >> 4) * 8 + e * 32, (unsigned)(64 * C_TB64(b) + 16 * w) * 1024u + C_DN(b) * 128, 0);
        }
      });
      const unsigned lane = opv(lane0), q = lane >> 4, r16 = lane & 15;
      const unsigned tb = opv(L_MOD + q * 16);
      f32x4 sa[4], c1[4];
#pragma unroll
      for (int i = 0; i < 4; ++i) { sa[i] = lds_f4(tb + i * 64); c1[i] = lds_f4(tb + 256 + i * 64); }
      const float rs0 = *(const LAS float*)(L_RS + (32 * w + r16) * 4), rs1 = *(const LAS float*)(L_RS + (32 * w + 16 + r16) * 4);
      SCHED();
      mm(31);
      SCHED();
      STAMP(4);
      const int b = ops(b0);
      const unsigned vo = ((q & 1) * 2 + (q >> 1)) * 256 + r16 * 16;
      const unsigned so = WS_PQ + (unsigned)((8 * C_TP(b) + 2 * w) * 32 + C_PART(b) * 16 + 2 * C_DN(b)) * 1024u;
#pragma unroll
      for (int tt = 0; tt < 2; ++tt) {
        const float rs = tt ? rs1 : rs0;
        unsigned pk[8];
#pragma unroll
        for (int i = 0; i < 4; ++i) {
          pk[2 * i] = pk2(fmaf(acc[tt][i][0], c1[i][0], rs * sa[i][0]), fmaf(acc[tt][i][1], c1[i][1], rs * sa[i][1]));
          pk[2 * i + 1] = pk2(fmaf(acc[tt][i][2], c1[i][2], rs * sa[i][2]), fmaf(acc[tt][i][3], c1[i][3], rs * sa[i][3]));
        }
#pragma unroll
        for (int pr = 0; pr < 2; ++pr)
          __builtin_amdgcn_raw_buffer_store_b128(merge16(pk[4 * pr], pk[4 * pr + 1], pk[4 * pr + 2], pk[4 * pr + 3]), rws, vo + pr * 1024, so + tt * 32768, 16);
      }
      WAITV(0);
      if (lane == 0) cnt_add(ws + WS_CPQ + (4 * C_TP(b) + w) * 256);
      STAMP(5);
    }

    float x1[16];
    {
      const int b = ops(b0);
      const unsigned lane = opv(lane0), q = lane >> 4, r16 = lane & 15, vl = lane * 16;
      const int tb64 = C_TB64(b), et = C_DN(b), tb16 = 4 * tb64 + w, t32c = 2 * tb64 + (w >> 1);
      lds_wait4(SY_WD);
      STAMP(15);
      cnt_wait(ws + WS_CPQ + t32c * 256, basePQ, lane, 1);
      STAMP(6);
      const int kh = w & 1;
      bf16x8 pq[32];
#pragma unroll
      for (int n = 0; n < 32; ++n)
        pq[n] = __builtin_bit_cast(bf16x8, __builtin_amdgcn_raw_buffer_load_b128(rws, vl + ((n >> 1) & 1) * 1024, WS_PQ + (unsigned)((2 * t32c + (n & 1)) * 32 + 16 * kh + (((n >> 2) + et) & 7) * 2) * 1024u, 16));
      SCHED();
      f32x4 y2[2][4] = {};
      bf16x8 F[64];
      unsigned fa;
      auto rdw = [&](int i) {
        if ((i & 7) == 0) {
          int nl = ((((i >> 3) + et) & 7) + 8 * kh - 4 * (tb64 & 3)) & 15;
          nl = nl >= 7 ? nl - 7 : nl + 12;
          fa = opv(vl + (unsigned)nl * 8192u);
        }
        F[i] = lds_frag(fa + (i & 7) * 1024);
      };
      static_for<0, 6>([&](auto ic) { rdw(decltype(ic)::value); });
      SCHED();
      static_for<0, 64>([&](auto ic) {
        constexpr int i = decltype(ic)::value;
        y2[0][i & 3] = MFMA16(F[i], pq[2 * (i >> 2)], y2[0][i & 3]);
        SCHED();
        y2[1][i & 3] = MFMA16(F[i], pq[2 * (i >> 2) + 1], y2[1][i & 3]);
        SCHED();
        if constexpr (i + 6 < 64) rdw(i + 6);
        if constexpr (i == PQ_SIG) { if (lane == 0) lds_inc(SY_PQL); }
        SCHED();
      });
      f32x4 y[4];
      pair_reduce(y, y2, 9 * 8192, SY_X3, w, lane);
      STAMP(7);
      const unsigned tb = opv(L_MOD + 512 + q * 16);
      f32x4 ga[4], c1[4];
#pragma unroll
      for (int e = 0; e < 4; ++e) { ga[e] = lds_f4(tb + e * 64); c1[e] = lds_f4(tb + 256 + e * 64); }
      unsigned pk[8];
      float sm = 0.f;
#pragma unroll
      for (int e = 0; e < 4; ++e) {
        x1[4 * e] = fmaf(ga[e][0], y[e][0], bflo(xr[e][0])); x1[4 * e + 1] = fmaf(ga[e][1], y[e][1], bfhi(xr[e][0]));
        x1[4 * e + 2] = fmaf(ga[e][2], y[e][2], bflo(xr[e][1])); x1[4 * e + 3] = fmaf(ga[e][3], y[e][3], bfhi(xr[e][1]));
        pk[2 * e] = pk2(x1[4 * e] * c1[e][0], x1[4 * e + 1] * c1[e][1]); pk[2 * e + 1] = pk2(x1[4 * e + 2] * c1[e][2], x1[4 * e + 3] * c1[e][3]);
        sm += (x1[4 * e] + x1[4 * e + 1]) + (x1[4 * e + 2] + x1[4 * e + 3]);
      }
      const unsigned vo = ((q & 1) * 2 + (q >> 1)) * 256 + r16 * 16;
#pragma unroll
      for (int pr = 0; pr < 2; ++pr)
        __builtin_amdgcn_raw_buffer_store_b128(merge16(pk[4 * pr], pk[4 * pr + 1], pk[4 * pr + 2], pk[4 * pr + 3]), rws, vo + pr * 1024, WS_AP + (unsigned)(tb16 * 16 + 2 * et) * 1024u, 16);
      const float mu = sum32(sum16(sm)) * (1.f / 64.f);
      float m2 = 0.f;
#pragma unroll
      for (int i = 0; i < 16; ++i) { const float d = x1[i] - mu; m2 = fmaf(d, d, m2); }
      m2 = sum32(sum16(m2));
      if (lane < 16) {
        const u32x2 o = {f2u(mu), f2u(m2)};
        __builtin_amdgcn_raw_buffer_store_b64(o, rws, lane * 8, WS_STATS + (unsigned)(et * 2048 + 16 * tb16) * 8u, 16);
      }
      if (lane == 0) lds_inc(SY_K3D);
      WAITV(0);
      if (lane == 0) cnt_add(ws + WS_CAP + t32c * 256);
      STAMP(8);
    }
    __builtin_amdgcn_s_setprio(0);
    {
      const int b = ops(b0);
      const unsigned lane = opv(lane0);
      bf16x8 W[32];
      lds_wait_ge(SY_AUX, 1);
      load_w(W, rws, 7 * C_FT(b) + w, C_LOC(b) >> 2, lane);
      k4_body(W, rws, ws, w, lane, C_TP4(b), C_FT(b), C_LOC(b) >> 2);
    }
    STAMP(9);

    {
      const int b = ops(b0);
      const unsigned lane = opv(lane0), q = lane >> 4, r16 = lane & 15, vl = lane * 16;
      const int tb64 = C_TB64(b), et = C_DN(b), tb16 = 4 * tb64 + w, t32c = 2 * tb64 + (w >> 1);
      cnt_wait(ws + WS_CG + t32c * 256, baseG, lane, 1);
      STAMP(10);
      const int kh = w & 1, rot5 = C_ROT5(b);
      const unsigned gb0 = WS_G + (unsigned)(2 * t32c) * 57344u + (unsigned)kh * 28672u;
      const unsigned vy = opv(vl + 57344);
      bf16x8 g[56];
      auto ldg = [&](int m) {
        int kb = m + rot5; kb = kb >= 14 ? kb - 14 : kb;
#pragma unroll
        for (int i = 0; i < 4; ++i)
          g[4 * m + i] = __builtin_bit_cast(bf16x8, __builtin_amdgcn_raw_buffer_load_b128(rws, ((i & 1) ? vy : vl) + (i >> 1) * 1024, gb0 + (unsigned)kb * 2048u, 16));
      };
      static_for<0, G_WIN>([&](auto mc) { ldg(decltype(mc)::value); });
      f32x4 y2[2][4] = {};
      bf16x8 F[112];
      unsigned fa;
      auto rdw = [&](int i) {
        if ((i & 7) == 0) {
          const int m = i >> 3;
          if (m == 5) lds_wait_ge(SY_WL + 1, 1);
          if (m == 9) lds_wait_ge(SY_WL + 2, 1);
          if (m == 13) lds_wait_ge(SY_WL + 3, 1);
          const int s0 = (16 + 2 * m) % 19, s1 = (17 + 2 * m) % 19;
          fa = opv(vl + (unsigned)(kh ? s1 : s0) * 8192u);
        }
        F[i] = lds_frag(fa + (i & 7) * 1024);
      };
      lds_wait_ge(SY_WL + 0, 1);
      static_for<0, 6>([&](auto ic) { rdw(decltype(ic)::value); });
      SCHED();
      static_for<0, 112>([&](auto ic) {
        constexpr int i = decltype(ic)::value;
        y2[0][i & 3] = MFMA16(F[i], g[2 * (i >> 2)], y2[0][i & 3]);
        SCHED();
        y2[1][i & 3] = MFMA16(F[i], g[2 * (i >> 2) + 1], y2[1][i & 3]);
        SCHED();
        if constexpr (i + 6 < 112) rdw(i + 6);
        if constexpr ((i & 7) == 7 && (i >> 3) + G_WIN < 14) ldg((i >> 3) + G_WIN);
        if constexpr (i == 31 || i == 47) { if (lane == 0) lds_inc(SY_PROG + (i == 47)); }
        if constexpr (i == 31 || i == 55 || i == 87) STAMP(24 + (i >> 5) + (i == 55));
        SCHED();
      });
      lds_wait_ge(SY_PROG + 1, 4);
      f32x4 y[4];
      pair_reduce(y, y2, 6 * 8192, SY_X5, w, lane);
      STAMP(11);
      const unsigned tb = opv(L_MOD + 1024 + q * 16);
      unsigned pk[8];
#pragma unroll
      for (int e = 0; e < 4; ++e) {
        const f32x4 gb = lds_f4(tb + e * 64);
        pk[2 * e] = pk2(fmaf(gb[0], y[e][0], x1[4 * e]), fmaf(gb[1], y[e][1], x1[4 * e + 1]));
        pk[2 * e + 1] = pk2(fmaf(gb[2], y[e][2], x1[4 * e + 2]), fmaf(gb[3], y[e][3], x1[4 * e + 3]));
      }
      const __amdgpu_buffer_rsrc_t ro = mk_rsrc(p.out, 2048u * 512u * 2u);
#pragma unroll
      for (int pr = 0; pr < 2; ++pr)
        __builtin_amdgcn_raw_buffer_store_b128(merge16(pk[4 * pr], pk[4 * pr + 1], pk[4 * pr + 2], pk[4 * pr + 3]), ro, r16 * 1024 + (q & 1) * 32 + (q >> 1) * 16 + pr * 64,
                                               (unsigned)(16 * tb16) * 1024u + et * 128, 0);
    }
    STAMP(12);
    if (b0 == 100 && wv == 0) { const unsigned t = (unsigned)__builtin_amdgcn_s_memrealtime(); while ((unsigned)__builtin_amdgcn_s_memrealtime() - t < 8u * (probe_t - probe_0)) __builtin_amdgcn_s_sleep(1); }
  }
}

#if defined(__HIP_DEVICE_COMPILE__)
#pragma clang attribute pop
#endif

extern "C" void kernel_launch(void* const* d_in, const int* in_sizes, int n_in, void* d_out, int out_size, void* d_ws, size_t ws_size,
                              hipStream_t stream) {
  Params p;
  p.x = (const u16*)d_in[0]; p.wsre = (const u16*)d_in[1]; p.wsim = (const u16*)d_in[2];
  p.wdre = (const u16*)d_in[3]; p.wdim = (const u16*)d_in[4];
  p.wg = (const u16*)d_in[6]; p.wu = (const u16*)d_in[7]; p.wdn = (const u16*)d_in[8];
  p.c = (const u16*)d_in[9]; p.wmod = (const u16*)d_in[10];
  p.out = (u16*)d_out; p.ws = (char*)d_ws;
  (void)hipFuncSetAttribute((const void*)fused_block, hipFuncAttributeMaxDynamicSharedMemorySize, LDS_BYTES);
  fused_block<<<256, 512, LDS_BYTES, stream>>>(p);
}
```

```cpp
#define XC 1
#include <hip/hip_runtime.h>

typedef unsigned short u16;
typedef unsigned long long u64;
typedef short bf16x8 __attribute__((ext_vector_type(8)));
typedef short s16x4 __attribute__((ext_vector_type(4)));
typedef __bf16 bf2 __attribute__((ext_vector_type(2)));
typedef float f32x16 __attribute__((ext_vector_type(16)));
typedef float f32x4 __attribute__((ext_vector_type(4)));
typedef float f32x2 __attribute__((ext_vector_type(2)));
typedef unsigned u32x4 __attribute__((ext_vector_type(4)));
typedef unsigned u32x2 __attribute__((ext_vector_type(2)));
#define DI __device__ __forceinline__
#define LAS __attribute__((address_space(3)))
#define GAS __attribute__((address_space(1)))

constexpr unsigned WS_NONCE = 0;
constexpr unsigned WS_FLAG = 131072;
constexpr unsigned WS_CPQ = 16384;
constexpr unsigned WS_CAP = 32768;
constexpr unsigned WS_CG = 49152;
constexpr unsigned WS_MODP = 65536;
constexpr unsigned WS_STATS = 393216;
constexpr unsigned WS_RSP = 524288;
constexpr unsigned WS_NT = 1u << 20;
constexpr unsigned WS_PQ = 4u << 20;
constexpr unsigned WS_AP = 8u << 20;
constexpr unsigned WS_G = 10u << 20;
constexpr unsigned WS_WT = 18u << 20;
constexpr unsigned WS_WDT = 22u << 20;
constexpr unsigned WS_WDH = 24u << 20;
constexpr unsigned WS_DIAG = 25u << 20;
constexpr unsigned WS_BYTES = 26u << 20;

constexpr unsigned L_STAGE = 24576;
constexpr unsigned L_AUX = 98304;
constexpr unsigned L_LN = 122880;
constexpr unsigned L_WM = 131840;
constexpr unsigned L_HK = 148224;
constexpr unsigned L_TAB = 155648;
constexpr unsigned L_MOD = L_TAB;
constexpr unsigned L_V = L_TAB + 1280;
constexpr unsigned L_ST = L_TAB + 3328;
constexpr unsigned L_SY = L_TAB + 4352;
constexpr unsigned LDS_BYTES = 163840;
enum { SY_ADONE = 0, SY_STAT = 4, SY_UFIN = 5, SY_GSIG = 9, SY_WL = 10, SY_PROG = 14, SY_LN = 16, SY_ND = 17, SY_AUX = 18, SY_PQL = 19, SY_WD = 20, SY_X3 = 24, SY_X5 = 28, SY_WIS = 32, SY_K3D = 33, SY_N = 34, SY_EP = 35, SY_BAP = 36 };
constexpr unsigned L_RS = L_TAB + 6400;
#define PRE_H 4
#define L_PACE 6
#define L_EARLY 26
#define G_WIN 4
#define W_SIG 21
#define H1 6
#define H2 16
#define PQ_SIG 40
#define W_PACE 4
#define WD_PACE 6

constexpr int SPIN_CAP = 1 << 20;
constexpr int CODE_LINES_PER_WG = 10;

struct Params {
  const u16 *x, *wsre, *wsim, *wdre, *wdim, *wg, *wu, *wdn, *c, *wmod;
  u16* out;
  char* ws;
};

#if defined(__HIP_DEVICE_COMPILE__)
#pragma clang attribute push(__attribute__((target("no-packed-fp32-ops"))), apply_to = function)
#endif
DI float u2f(unsigned u) { return __builtin_bit_cast(float, u); }
DI unsigned f2u(float f) { return __builtin_bit_cast(unsigned, f); }
DI bool all_lanes(bool c) { return __builtin_amdgcn_ballot_w64(c) == ~0ull; }
DI u32x4 mk_srd(const void* p, unsigned bytes) {
  u64 a = (u64)p;
  u32x4 r = {(unsigned)a, (unsigned)(a >> 32) & 0xffffu, bytes, 0x00020000u};
  return r;
}
DI __amdgpu_buffer_rsrc_t mk_rsrc(const void* p, unsigned bytes) {
  return __builtin_amdgcn_make_buffer_rsrc((void*)p, 0, bytes, 0x00020000);
}
#define RFL(x) __builtin_amdgcn_readfirstlane(x)
#define BAR() __builtin_amdgcn_s_barrier()
#define WAITV(n) asm volatile("s_waitcnt vmcnt(%0)" ::"i"(n) : "memory")
#define WAITL0() __builtin_amdgcn_s_waitcnt(0xC07F)
#define CFENCE() asm volatile("" ::: "memory")

DI void dma4v(u32x4 srd, unsigned v0, unsigned v1, unsigned v2, unsigned v3, unsigned soff, unsigned m0v) {
  asm volatile("s_mov_b32 m0, %0\n\ts_nop 0\n\t"
               "buffer_load_dwordx4 %1, %5, %6 offen lds\n\t"
               "buffer_load_dwordx4 %2, %5, %6 offen offset:1024 lds\n\t"
               "buffer_load_dwordx4 %3, %5, %6 offen offset:2048 lds\n\t"
               "buffer_load_dwordx4 %4, %5, %6 offen offset:3072 lds"
               ::"s"(m0v), "v"(v0), "v"(v1), "v"(v2), "v"(v3), "s"(srd), "s"(soff) : "memory");
}
DI void dma2v(u32x4 srd, unsigned v0, unsigned v1, unsigned soff, unsigned m0v) {
  asm volatile("s_mov_b32 m0, %0\n\ts_nop 0\n\t"
               "buffer_load_dwordx4 %1, %3, %4 offen lds\n\t"
               "buffer_load_dwordx4 %2, %3, %4 offen offset:1024 lds"
               ::"s"(m0v), "v"(v0), "v"(v1), "s"(srd), "s"(soff) : "memory");
}
DI void dma2n(u32x4 srd, unsigned v0, unsigned v1, unsigned soff, unsigned m0v) {
  asm volatile("s_mov_b32 m0, %0\n\ts_nop 0\n\t"
               "buffer_load_dwordx4 %1, %3, %4 offen nt lds\n\t"
               "buffer_load_dwordx4 %2, %3, %4 offen offset:1024 nt lds"
               ::"s"(m0v), "v"(v0), "v"(v1), "s"(srd), "s"(soff) : "memory");
}
DI void dma2c(u32x4 srd, unsigned v, unsigned soff, unsigned m0v) {
  asm volatile("s_mov_b32 m0, %0\n\ts_nop 0\n\t"
               "buffer_load_dwordx4 %1, %2, %3 offen sc1 lds\n\t"
               "buffer_load_dwordx4 %1, %2, %3 offen offset:1024 sc1 lds"
               ::"s"(m0v), "v"(v), "s"(srd), "s"(soff) : "memory");
}
DI void dma4c(u32x4 srd, unsigned v, unsigned soff, unsigned m0v) {
  asm volatile("s_mov_b32 m0, %0\n\ts_nop 0\n\t"
               "buffer_load_dwordx4 %1, %2, %3 offen sc1 lds\n\t"
               "buffer_load_dwordx4 %1, %2, %3 offen offset:1024 sc1 lds\n\t"
               "buffer_load_dwordx4 %1, %2, %3 offen offset:2048 sc1 lds\n\t"
               "buffer_load_dwordx4 %1, %2, %3 offen offset:3072 sc1 lds"
               ::"s"(m0v), "v"(v), "s"(srd), "s"(soff) : "memory");
}
DI void dma1w(u32x4 srd, unsigned v, unsigned soff, unsigned m0v) {
  asm volatile("s_mov_b32 m0, %0\n\ts_nop 0\n\tbuffer_load_dword %1, %2, %3 offen sc1 lds" ::"s"(m0v), "v"(v), "s"(srd), "s"(soff) : "memory");
}
DI void dma1c(u32x4 srd, unsigned v, unsigned soff, unsigned m0v) {
  asm volatile("s_mov_b32 m0, %0\n\ts_nop 0\n\tbuffer_load_dwordx4 %1, %2, %3 offen sc1 lds" ::"s"(m0v), "v"(v), "s"(srd), "s"(soff) : "memory");
}
DI unsigned opv(unsigned v) { asm volatile("" : "+v"(v)); return v; }
DI int ops(int v) { asm volatile("" : "+s"(v)); return v; }
#define SCHED() __builtin_amdgcn_sched_barrier(0)
#define SGB(m, n) __builtin_amdgcn_sched_group_barrier((m), (n), 0)
DI bf16x8 lds_frag(unsigned a) { return *(const LAS bf16x8*)a; }
DI f32x4 lds_f4(unsigned a) { return *(const LAS f32x4*)a; }
DI bf16x8 lds_tr2(unsigned a0, unsigned a1) {
  s16x4 lo = __builtin_amdgcn_ds_read_tr16_b64_v4i16((LAS s16x4*)a0);
  s16x4 hi = __builtin_amdgcn_ds_read_tr16_b64_v4i16((LAS s16x4*)a1);
  return __builtin_shufflevector(lo, hi, 0, 1, 2, 3, 4, 5, 6, 7);
}
DI unsigned pk2(float a, float b) { bf2 p = {(__bf16)a, (__bf16)b}; return __builtin_bit_cast(unsigned, p); }
DI float bflo(unsigned u) { return u2f(u << 16); }
DI float bfhi(unsigned u) { return u2f(u & 0xffff0000u); }
DI float rbf(float a) { return (float)(__bf16)a; }
DI float sum32(float v) { auto r = __builtin_amdgcn_permlane32_swap(f2u(v), f2u(v), false, false); return u2f(r[0]) + u2f(r[1]); }
DI float sum16(float v) { auto r = __builtin_amdgcn_permlane16_swap(f2u(v), f2u(v), false, false); return u2f(r[0]) + u2f(r[1]); }
template <int CTRL> DI float dpp_add(float v) { return v + u2f(__builtin_amdgcn_update_dpp(0u, f2u(v), CTRL, 0xf, 0xf, true)); }
DI float wave_sum(float v) {
  v = dpp_add<0xB1>(v);
  v = dpp_add<0x4E>(v);
  v = dpp_add<0x141>(v);
  v = dpp_add<0x140>(v);
  return sum32(sum16(v));
}
#define MFMA16(a, b, c) __builtin_amdgcn_mfma_f32_16x16x32_bf16((a), (b), (c), 0, 0, 0)

DI unsigned atomic_peek(const char* p) {
  unsigned r, z = 0;
  asm volatile("global_atomic_add %0, %1, %2, off sc0\n\ts_waitcnt vmcnt(0)" : "=&v"(r) : "v"(p), "v"(z) : "memory");
  return r;
}
DI void cnt_add(char* p) { asm volatile("global_atomic_add %0, %1, off" ::"v"(p), "v"(1u) : "memory"); }
DI unsigned flag_ld(const char* p) { return __hip_atomic_load((GAS unsigned*)p, __ATOMIC_RELAXED, __HIP_MEMORY_SCOPE_AGENT); }
DI void flag_st(char* p, unsigned v) { asm volatile("global_atomic_swap %0, %1, off" ::"v"(p), "v"(v) : "memory"); }
DI void round_set(char* ws, int round, int b, unsigned lane, unsigned epoch) {
  if (lane < 16) flag_st(ws + WS_FLAG + round * 16384 + lane * 1024 + (((b >> 1) & 3) * 64 + (b >> 3) * 2 + (b & 1)) * 4, epoch);
}
DI const char* round_word(const char* ws, int round, int b, int quarter, unsigned lane) { return ws + WS_FLAG + round * 16384 + ((b >> 3) & 15) * 1024 + (quarter * 64 + lane) * 4; }
DI void cnt_wait(const char* p, unsigned base, int lane, int nl) {
_Pragma("unroll 1") for (int sp = 0; sp < SPIN_CAP; ++sp) {
    unsigned v = base + 16u;
    if (lane < nl) v = atomic_peek(p);
    if (all_lanes(v - base >= 16u)) break;
  }
  CFENCE();
}
DI void lds_wait_ge(unsigned word, unsigned want) {
  volatile LAS unsigned* p = (volatile LAS unsigned*)(L_SY + word * 4);
_Pragma("unroll 1") for (int sp = 0; sp < SPIN_CAP; ++sp) {
    if (RFL(*p) >= want) break;
    __builtin_amdgcn_s_sleep(1);
  }
  CFENCE();
}
DI void lds_wait4(unsigned word) {
  volatile LAS u32x4* p = (volatile LAS u32x4*)(L_SY + word * 4);
_Pragma("unroll 1") for (int sp = 0; sp < SPIN_CAP; ++sp) {
    const u32x4 v = *p;
    if (RFL(v[0] & v[1] & v[2] & v[3])) break;
    __builtin_amdgcn_s_sleep(1);
  }
  CFENCE();
}
DI unsigned lds_get(unsigned word) { return *(volatile LAS unsigned*)(L_SY + word * 4); }
DI void lds_set(unsigned word, unsigned v) { *(volatile LAS unsigned*)(L_SY + word * 4) = v; }
DI unsigned lds_inc(unsigned word) { return __hip_atomic_fetch_add((LAS unsigned*)(L_SY + word * 4), 1u, __ATOMIC_RELAXED, __HIP_MEMORY_SCOPE_WORKGROUP); }

#define STAMP(i)
#define STAMPB(i)

template <int I> struct IC { static constexpr int value = I; };
template <int A, int B, class F> DI void static_for(F&& f) { if constexpr (A < B) { f(IC<A>{}); static_for<A + 1, B>(f); } }

DI u32x4 merge16(unsigned a0, unsigned a1, unsigned b0, unsigned b1) {
  auto s0 = __builtin_amdgcn_permlane16_swap(a0, b0, false, false);
  auto s1 = __builtin_amdgcn_permlane16_swap(a1, b1, false, false);
  const u32x4 o = {s0[0], s1[0], s0[1], s1[1]};
  return o;
}

DI void pair_reduce(f32x4 (&y)[4], const f32x4 (&y2)[2][4], unsigned scratch, unsigned word, int w, unsigned lane) {
  const bool odd = w & 1;
  const unsigned a = scratch + lane * 16;
#pragma unroll
  for (int e = 0; e < 4; ++e) *(LAS f32x4*)(a + w * 4096 + e * 1024) = odd ? y2[0][e] : y2[1][e];
  CFENCE();
  lds_set(word + w, 1);
  lds_wait_ge(word + (w ^ 1), 1);
#pragma unroll
  for (int e = 0; e < 4; ++e) y[e] = (odd ? y2[1][e] : y2[0][e]) + lds_f4(a + (w ^ 1) * 4096 + e * 1024);
}

DI void load_w(bf16x8 (&W)[32], __amdgpu_buffer_rsrc_t rws, int c, int rot, unsigned lane) {
  const unsigned vl = opv(lane * 16);
#pragma unroll
  for (int k = 0; k < 32; ++k) {
    W[k] = __builtin_bit_cast(bf16x8, __builtin_amdgcn_raw_buffer_load_b128(rws, vl + (k & 3) * 1024, WS_WT + (unsigned)(c * 32 + (((k >> 2) + rot) & 7) * 4) * 1024u, 16));
    if ((k & 1) == 1 && k >= W_PACE && k < 31) WAITV(W_PACE);
    if (k == W_SIG && lane == 0) lds_inc(SY_WIS);
  }
  SCHED();
}
template <int N> DI float row_bcast(float v) { return u2f(__builtin_amdgcn_update_dpp(0u, f2u(v), 0x150 + N, 0xf, 0xf, true)); }

DI float geglu(float g, float u, float rstd, float nmr, float ug, float vg, float uu, float vu) {
  const float a = fmaf(g, rstd, fmaf(nmr, ug, vg));
  const float b = fmaf(u, rstd, fmaf(nmr, uu, vu));
  const float e = a * fmaf(a * a, -0.10294324f, -2.30220820f);
  return a * b * __builtin_amdgcn_rcpf(1.f + __builtin_amdgcn_exp2f(e));
}

DI void k4_body(const bf16x8 (&W)[32], __amdgpu_buffer_rsrc_t rws, char* ws, int wv, unsigned lane, int tp4, int ft, int rot) {
  const unsigned q = lane >> 4, r16 = lane & 15;
  const int c = 7 * ft + wv;
  f32x4 ug, vg, uu, vu;
  {
    f32x4 dg = {}, du = {};
    const unsigned va = L_V + (lane & 1) * 1024 + q * 16;
#pragma unroll
    for (int k = 0; k < 16; ++k) {
      const bf16x8 f = lds_frag(va + (unsigned)((((k >> 1) + rot) & 7) * 128 + (k & 1) * 64));
      dg = MFMA16(W[2 * k], f, dg); du = MFMA16(W[2 * k + 1], f, du);
    }
#pragma unroll
    for (int j = 0; j < 4; ++j) { vg[j] = row_bcast<0>(dg[j]); ug[j] = row_bcast<1>(dg[j]); vu[j] = row_bcast<0>(du[j]); uu[j] = row_bcast<1>(du[j]); }
  }
  SCHED();
  const unsigned gvoff = (q & 1) * 57344 + (c >> 1) * 1024 + (c & 1) * 512 + (q >> 1) * 256 + r16 * 16;
  const unsigned sb = opv(L_ST + r16 * 8);
  unsigned ab[8];
#pragma unroll
  for (int g = 0; g < 8; ++g) ab[g] = lane * 16 + (unsigned)(((g + rot) & 7) * 2048);
  lds_wait_ge(SY_ADONE + 0, 1);
#pragma unroll 1
  for (int u = 0; u < 4; ++u) {
    const int tbl = (u + ft) & 3;
    STAMP(16 + 2 * u);
    f32x4 gx = {}, ux = {}, gy = {}, uy = {};
    bf16x8 A[32];
    auto rda = [&](int i) { A[i] = lds_frag(ab[i >> 2] + ((i >> 1) & 1) * 1024 + (i & 1) * 16384); };
    static_for<0, 6>([&](auto ic) { rda(decltype(ic)::value); });
    unsigned nxt, sto;
    SCHED();
    static_for<0, 32>([&](auto ic) {
      constexpr int i = decltype(ic)::value;
      if constexpr (i & 1) { gy = MFMA16(W[i - 1], A[i], gy); SCHED(); uy = MFMA16(W[i], A[i], uy); }
      else { gx = MFMA16(W[i], A[i], gx); SCHED(); ux = MFMA16(W[i + 1], A[i], ux); }
      SCHED();
      if constexpr (i + 6 < 32) rda(i + 6);
      if constexpr (i == 26) nxt = lds_get(SY_ADONE + u + 1);
      if constexpr (i == 27) sto = lds_get(SY_STAT);
      SCHED();
    });
    STAMP(17 + 2 * u);
    if (lane == 0) lds_inc(SY_UFIN + u);
    if (!RFL(sto)) lds_wait_ge(SY_STAT, 1);
    const f32x2 sx = *(const LAS f32x2*)(sb + tbl * 256), sy = *(const LAS f32x2*)(sb + tbl * 256 + 128);
#pragma unroll
    for (int g = 0; g < 8; ++g) ab[g] += 32768;
    float ox[4], oy[4];
#pragma unroll
    for (int j = 0; j < 4; ++j) {
      ox[j] = geglu(gx[j], ux[j], sx[0], sx[1], ug[j], vg[j], uu[j], vu[j]);
      oy[j] = geglu(gy[j], uy[j], sy[0], sy[1], ug[j], vg[j], uu[j], vu[j]);
    }
    __builtin_amdgcn_raw_buffer_store_b128(merge16(pk2(ox[0], ox[1]), pk2(ox[2], ox[3]), pk2(oy[0], oy[1]), pk2(oy[2], oy[3])), rws, gvoff,
                                           WS_G + (unsigned)(2 * (4 * tp4 + tbl)) * 57344u, 16);
    if (!RFL(nxt)) lds_wait_ge(SY_ADONE + u + 1, 1);
  }
  WAITV(0);
  unsigned old = 0;
  if (lane == 0) old = lds_inc(SY_GSIG);
  if (RFL(old) == 6 && lane < 4) cnt_add(ws + WS_CG + (4 * tp4 + lane) * 256);
}

#define C_XCD(b) ((b) & 7)
#define C_LOC(b) ((b) >> 3)
#define C_TP(b) (2 * C_XCD(b) + (C_LOC(b) >> 4))
#define C_PART(b) ((C_LOC(b) >> 3) & 1)
#define C_DN(b) (C_LOC(b) & 7)
#define C_J0(b) (4 * ((C_DN(b) + C_XCD(b) + 4 * C_PART(b)) & 7))
#define C_TB64(b) (4 * C_XCD(b) + (C_LOC(b) >> 3))
#define C_TP4(b) (8 * (C_XCD(b) & 1) + (C_LOC(b) >> 2))
#define C_FT(b) (4 * (C_XCD(b) >> 1) + (C_LOC(b) & 3))
#define C_ROT5(b) ((7 * C_DN(b) / 4 + 7 * (C_TB64(b) & 3) / 2) % 14)

__global__ __launch_bounds__(512, 2) void fused_block(Params p) {
  const int b0 = blockIdx.x;
  const unsigned lane0 = threadIdx.x & 63;
  const int wv = RFL(threadIdx.x >> 6);
  char* const ws = p.ws;
  u64 pc0;
  asm volatile("s_getpc_b64 %0" : "=s"(pc0));
  STAMP(0);
  const u32x4 sws = mk_srd(ws, WS_BYTES);
  const __amdgpu_buffer_rsrc_t rws = mk_rsrc(ws, WS_BYTES);

  if (wv >= 4) {
    const int wl = wv - 4;
    const unsigned lane = lane0;
    const unsigned sw = (lane & 7) ^ (lane >> 4);
    const unsigned vl = lane * 16;
    {
      const int b = b0, j0 = C_J0(b), dn = C_DN(b);
      unsigned junk = 0;
      if (wv == 4 && lane < CODE_LINES_PER_WG)
        asm volatile("global_load_dword %0, %1, off" : "=&v"(junk) : "v"((const char*)(pc0 & ~255ull) + (C_LOC(b) * CODE_LINES_PER_WG + lane) * 128) : "memory");
      const u32x4 sA = mk_srd(C_PART(b) ? p.wsim : p.wsre, 2048u * 2048u * 2u);
      const unsigned ra = (unsigned)(128 * C_TP(b) + 32 * wl + (lane >> 3)) * 4096u;
      const unsigned va0 = ra + sw * 16, va1 = ra + 8 * 4096 + (sw ^ 4) * 16 - 1024, va2 = ra + 16 * 4096 + sw * 16 - 2048, va3 = ra + 24 * 4096 + (sw ^ 4) * 16 - 3072;
      const unsigned o1 = (lane & 11) == 0 ? 0x3f803f80u : 0u;
      const u32x4 ones4 = {o1, o1, o1, o1};
      const bf16x8 sel = __builtin_bit_cast(bf16x8, ones4);
      const unsigned wa = (32 * wl + (lane & 15)) * 128 + (((lane >> 4) ^ ((lane >> 1) & 7)) * 16);
      f32x4 r0 = {}, r1 = {};
      auto rsum = [&](int s) {
        const unsigned a = wa + s * L_STAGE;
        r0 = MFMA16(sel, lds_frag(a), r0); r1 = MFMA16(sel, lds_frag(a + 2048), r1);
        r0 = MFMA16(sel, lds_frag(a ^ 64), r0); r1 = MFMA16(sel, lds_frag((a ^ 64) + 2048), r1);
      };
      auto half = [&](int hs) {
        if (hs & 1) dma2n(sA, va2 + 2048, va3 + 2048, (unsigned)(j0 + (hs >> 1)) * 128u, (hs >> 1) * L_STAGE + wl * 4096 + 2048);
        else dma2n(sA, va0, va1, (unsigned)(j0 + (hs >> 1)) * 128u, (hs >> 1) * L_STAGE + wl * 4096);
      };
      half(0);
      BAR();
#pragma unroll
      for (int hs = 1; hs < PRE_H; ++hs) half(hs);
      STAMP(1);
      BAR();
      asm volatile("" : "+v"(junk));
#pragma unroll
      for (int hs = PRE_H; hs < 8; ++hs) half(hs);
      asm volatile("s_waitcnt vmcnt(12)" : "+v"(junk)::"memory");
      BAR();
      STAMP(2);
      rsum(0);
      WAITV(8); BAR();
      STAMPB(1);
      auto stage1 = [&](int s) {
        const unsigned j = (unsigned)(j0 + s) & 31u, sl = (unsigned)(s & 3) * L_STAGE;
        WAITV(L_PACE);
        dma2c(sws, vl, WS_NT + ((unsigned)dn * 32u + j) * 8192u + wl * 2048, sl + 16384 + wl * 2048);
      };
      auto stage2 = [&](int s) {
        const unsigned j = (unsigned)(j0 + s) & 31u, sl = (unsigned)(s & 3) * L_STAGE;
        WAITV(L_PACE);
        dma2v(sA, va0, va1, j * 128u, sl + wl * 4096);
        WAITV(L_PACE);
        dma2v(sA, va2 + 2048, va3 + 2048, j * 128u, sl + wl * 4096 + 2048);
      };
      auto stage = [&](int s) { stage1(s); stage2(s); };
      rsum(1);
      WAITL0();
      stage(4);
      WAITV(10); BAR();
      STAMPB(2);
      rsum(2);
      stage(5);
      WAITV(12); BAR();
      STAMPB(3);
      rsum(3);
      if (lane < 32) __builtin_amdgcn_raw_buffer_store_b32(f2u(lane < 16 ? r0[0] : r1[0]), rws, lane * 4, WS_RSP + (unsigned)((dn * 2 + C_PART(b)) * 2048 + 128 * C_TP(b) + 32 * wl) * 4u, 16);
      stage(6);
      WAITV(12); BAR();
      STAMPB(4);
#pragma unroll 1
      for (int s = 5; s < L_EARLY; ++s) { stage(s + 2); WAITV(12); BAR(); STAMPB(s); if ((s & 3) == 0) STAMP(23 + (s >> 2)); }
#pragma unroll 1
      for (int s = L_EARLY; s <= 29; ++s) { stage1(s + 2); WAITV(8); BAR(); STAMPB(s); if ((s & 3) == 0) STAMP(23 + (s >> 2)); stage2(s + 2); }
    }
    const int b = ops(b0);
    {
      auto wdblk = [&](int n) {
        const unsigned blk = (unsigned)(n + 4 * (C_TB64(b) & 3)) & 15u;
        dma2c(sws, vl, WS_WDH + ((unsigned)C_DN(b) * 16u + blk) * 8192u + wl * 2048, (unsigned)(n < 7 ? 12 + n : n - 7) * 8192u + wl * 2048);
      };
      WAITV(6); BAR();
      STAMPB(30);
      WAITV(4); wdblk(0); WAITV(4); wdblk(1);
      WAITV(4); BAR();
      STAMP(3);
#pragma unroll 1
      for (int n = 2; n < 16; ++n) { WAITV(WD_PACE); wdblk(n); if ((n & 3) == 0) STAMP(9 + (n >> 2)); }
    }
    const int tp4 = C_TP4(b), ft = C_FT(b), rot4 = C_LOC(b) >> 2;
    if (wv < 7) {
      bf16x8 W[32];
      WAITV(0);
      lds_set(SY_WD + wl, 1);
      STAMP(4);
      lds_wait_ge(SY_AUX, 1);
      lds_wait_ge(SY_PQL, 4);
      load_w(W, rws, 7 * ft + wv, rot4, lane);
      STAMP(5);
      k4_body(W, rws, ws, wv, lane, tp4, ft, rot4);
      STAMP(8);
    } else {
      WAITV(0);
      lds_set(SY_WD + 3, 1);
      STAMP(4);
      {
        const unsigned ep = RFL(lds_get(SY_EP));
        const char* fp = round_word(ws, 2, b, 0, lane);
_Pragma("unroll 1") for (int sp = 0; sp < SPIN_CAP; ++sp) {
          unsigned f0, f1, f2, f3;
          asm volatile("global_atomic_add %0, %4, %5, off sc0\n\tglobal_atomic_add %1, %4, %5, off offset:256 sc0\n\tglobal_atomic_add %2, %4, %5, off offset:512 sc0\n\t"
                       "global_atomic_add %3, %4, %5, off offset:768 sc0\n\ts_waitcnt vmcnt(0)"
                       : "=&v"(f0), "=&v"(f1), "=&v"(f2), "=&v"(f3) : "v"(fp), "v"(0u) : "memory");
          if (all_lanes(f0 == ep && f1 == ep && f2 == ep && f3 == ep)) break;
          __builtin_amdgcn_s_sleep(12);
        }
        lds_set(SY_AUX, 1);
        STAMP(9);
      }
      const unsigned baseAP = lds_get(SY_BAP + (lane & 3));
      lds_wait_ge(SY_K3D, 4);
      STAMP(5);
      const int rot5 = C_ROT5(b), et = C_DN(b);
      auto wdn = [&](int n0, int n1) {
#pragma unroll 1
        for (int n = n0; n < n1; ++n) {
          int kb = (n >> 1) + rot5; kb = (kb >= 14 ? kb - 14 : kb) + 14 * (n & 1);
          int sn = n + 16; sn = sn >= 38 ? sn - 38 : sn >= 19 ? sn - 19 : sn;
          const unsigned so = WS_WDT + (unsigned)(et * 28 + kb) * 8192u, sl = (unsigned)sn * 8192u;
          dma4c(sws, vl, so, sl); dma4c(sws, vl, so + 4096, sl + 4096);
          WAITV(8);
        }
      };
      lds_wait_ge(SY_WIS, 7);
      STAMP(13);
      cnt_wait(ws + WS_CAP + (4 * tp4 + lane) * 256, baseAP, lane, 4);
      STAMP(6);
      auto ablk = [&](int h0, int h1) {
#pragma unroll 1
        for (int hh = h0; hh < h1; ++hh) {
          const unsigned so = WS_AP + (unsigned)(4 * tp4 + (((hh >> 1) + ft) & 3)) * 32768u + (hh & 1) * 16384, sl = (unsigned)hh * 16384u;
#pragma unroll
          for (int q = 0; q < 4; ++q) dma4c(sws, vl, so + q * 4096, sl + q * 4096);
        }
      };
      ablk(0, 3);
      WAITV(16); lds_set(SY_ADONE + 0, 1);
      STAMP(7);
      ablk(3, 4);
      u32x2 sp[2][8];
#pragma unroll
      for (int hf = 0; hf < 2; ++hf)
#pragma unroll
        for (int e = 0; e < 8; ++e)
          sp[hf][e] = __builtin_amdgcn_raw_buffer_load_b64(rws, lane * 8 + hf * 512, WS_STATS + (unsigned)(e * 2048 + 128 * tp4) * 8u, 16);
      CFENCE();
      WAITV(16); lds_set(SY_ADONE + 1, 1);
      STAMP(16);
#pragma unroll
      for (int hf = 0; hf < 2; ++hf) {
        float mu = 0.f, m2 = 0.f;
#pragma unroll
        for (int e = 0; e < 8; ++e) { mu += u2f(sp[hf][e][0]); m2 += u2f(sp[hf][e][1]); }
        mu *= 0.125f;
        float dv = 0.f;
#pragma unroll
        for (int e = 0; e < 8; ++e) { const float d = u2f(sp[hf][e][0]) - mu; dv = fmaf(d, d, dv); }
        const float rstd = __builtin_amdgcn_rsqf(fmaf(dv, 64.f, m2) * (1.f / 512.f) + 1e-5f);
        const f32x2 o = {rstd, -mu * rstd};
        *(LAS f32x2*)(L_ST + (64 * hf + lane) * 8) = o;
      }
      WAITL0(); lds_set(SY_STAT, 1);
      STAMP(17);
      ablk(4, 7);
      WAITV(16); lds_set(SY_ADONE + 2, 1);
      STAMP(18);
      ablk(7, 8);
      WAITV(0); lds_set(SY_ADONE + 3, 1);
      STAMP(19);
      wdn(0, 3);
      lds_wait_ge(SY_UFIN + 0, 7); wdn(3, 7);
      lds_wait_ge(SY_UFIN + 1, 7); wdn(7, 11);
      lds_set(SY_WL + 0, 1);
      STAMP(20);
      lds_wait_ge(SY_UFIN + 2, 7); wdn(11, 15);
      lds_wait_ge(SY_UFIN + 3, 7); wdn(15, 19);
      WAITV(0); lds_set(SY_WL + 1, 1);
      STAMP(8);
      lds_wait_ge(SY_PROG + 0, 4);
      wdn(19, 27);
      lds_set(SY_WL + 2, 1);
      STAMP(21);
      lds_wait_ge(SY_PROG + 1, 4);
      wdn(27, 28);
      WAITV(0); lds_set(SY_WL + 3, 1);
      STAMP(22);
    }
  } else {
    const int w = wv;
    unsigned epoch, basePQ, baseG;
    {
      const int b = b0;
      const unsigned lane = lane0, q = lane >> 4, r16 = lane & 15;
      u32x4 xa, xb;
      asm volatile("global_load_dwordx4 %0, %2, off nt\n\tglobal_load_dwordx4 %1, %2, off offset:1024 nt" : "=&v"(xa), "=&v"(xb) : "v"((const char*)p.x + (8 * b + 2 * w) * 1024 + lane * 16) : "memory");
      unsigned sv = 0, ev;
      STAMP(28);
      asm volatile("global_load_dword %0, %1, off sc1" : "=&v"(ev) : "v"(ws + WS_NONCE) : "memory");
      {
        const unsigned off = lane < 2 ? (lane ? WS_CG : WS_CPQ) + (2 * C_TB64(b) + (w >> 1)) * 256 : WS_CAP + (4 * C_TP4(b) + lane - 2) * 256;
        if (lane < (w == 0 ? 6u : 2u)) asm volatile("global_atomic_add %0, %1, %2, off sc0" : "=&v"(sv) : "v"(ws + off), "v"(0u) : "memory");
      }
      if (threadIdx.x < SY_N) lds_set(threadIdx.x, 0);
      WAITL0(); BAR();
      __builtin_amdgcn_s_setprio(1);
      asm volatile("s_waitcnt vmcnt(2)" : "+v"(xa), "+v"(xb)::"memory");
      STAMP(13);
      {
        const bf2 one = __builtin_bit_cast(bf2, 0x3f803f80u);
        float sa = 0.f, sb = 0.f, qa = 0.f, qb = 0.f;
#pragma unroll
        for (int i = 0; i < 4; ++i) {
          const unsigned ua = xa[i], ub = xb[i];
          const bf2 pa = __builtin_bit_cast(bf2, ua), pb = __builtin_bit_cast(bf2, ub);
          sa = __builtin_amdgcn_fdot2_f32_bf16(pa, one, sa, false); qa = __builtin_amdgcn_fdot2_f32_bf16(pa, pa, qa, false);
          sb = __builtin_amdgcn_fdot2_f32_bf16(pb, one, sb, false); qb = __builtin_amdgcn_fdot2_f32_bf16(pb, pb, qb, false);
        }
        sa = wave_sum(sa) * (1.f / 512.f); sb = wave_sum(sb) * (1.f / 512.f);
        qa = wave_sum(qa) * (1.f / 512.f); qb = wave_sum(qb) * (1.f / 512.f);
        qa = __builtin_amdgcn_rsqf(__builtin_fmaxf(fmaf(-sa, sa, qa), 0.f) + 1e-5f); qb = __builtin_amdgcn_rsqf(__builtin_fmaxf(fmaf(-sb, sb, qb), 0.f) + 1e-5f);
        sa *= -qa; sb *= -qb;
        u32x4 na, nb;
#pragma unroll
        for (int i = 0; i < 4; ++i) {
          na[i] = pk2(fmaf(bflo(xa[i]), qa, sa), fmaf(bfhi(xa[i]), qa, sa));
          nb[i] = pk2(fmaf(bflo(xb[i]), qb, sb), fmaf(bfhi(xb[i]), qb, sb));
        }
        *(LAS u32x4*)(L_LN + (2 * w) * 1088 + lane * 16) = na;
        *(LAS u32x4*)(L_LN + (2 * w + 1) * 1088 + lane * 16) = nb;
      }
      CFENCE();
      if (lane == 0) lds_inc(SY_LN);
      lds_wait_ge(SY_LN, 4);
      {
        const unsigned la = L_LN + (r16 >> 2) * 1088 + (128 * w + 16 * q + 4 * (lane & 3)) * 2;
#pragma unroll
        for (int i = 0; i < 2; ++i) {
          const bf16x8 f = lds_tr2(la + i * 128, la + i * 128 + 4 * 1088);
          __builtin_amdgcn_raw_buffer_store_b128(__builtin_bit_cast(u32x4, f), rws, lane * 16, WS_NT + (unsigned)((2 * w + i) * 256 + b) * 1024u, 16);
        }
      }
      STAMP(14);
      asm volatile("s_waitcnt vmcnt(0)" : "+v"(sv), "+v"(ev)::"memory");
      basePQ = __builtin_amdgcn_readlane(sv, 0); baseG = __builtin_amdgcn_readlane(sv, 1);
      epoch = RFL(ev) + 1u;
      unsigned old = 0;
      if (lane == 0) old = lds_inc(SY_ND);
      if (RFL(old) == 3) round_set(ws, 0, b, lane, epoch);
      STAMP(1);
      if (w == 0) { if (lane >= 2 && lane < 6) lds_set(SY_BAP + lane - 2, sv); if (lane == 6) lds_set(SY_EP, epoch); }
      {
        const char* fp = round_word(ws, 0, b, w, lane);
_Pragma("unroll 1") for (int sp = 0; sp < SPIN_CAP; ++sp)
          if (all_lanes(atomic_peek(fp) == epoch)) break;
      }
#pragma unroll
      for (int s = 0; s < 4; ++s) dma2c(sws, lane * 16, WS_NT + (unsigned)(C_DN(b) * 32 + C_J0(b) + s) * 8192u + w * 2048, s * L_STAGE + 16384 + w * 2048);
      BAR();
      STAMP(2);
      const unsigned m0 = (unsigned)(128 * (b & 3) + 32 * w + (lane >> 3)) * 6144u + (lane & 7) * 16 + (b < 192 ? (b >> 2) * 128 : 0x40000000);
      const u32x4 sM = mk_srd(p.wmod, 512u * 3072u * 2u);
      dma2n(sM, m0, m0 + 8 * 6144 - 1024, 0, L_WM + w * 4096);
      dma2n(sM, m0 + 16 * 6144, m0 + 24 * 6144 - 1024, 0, L_WM + w * 4096 + 2048);
      dma1w(mk_srd(p.c, 1024u), lane * 4, (b & 3) * 256, L_HK + w * 256);
      const unsigned h0 = (unsigned)(64 * (b & 7) + 16 * w + r16) * 1024u + q * 16 + (b >= 128 ? 0 : 0x40000000);
      lds_wait_ge(SY_ND, 4);
      dma2n(mk_srd(b >= 192 ? p.wdim : p.wdre, 512u * 512u * 2u), h0, h0 + 64 - 1024, ((b >> 3) & 7) * 128, L_LN + w * 2048);
    }

    u32x2 xr[4];
    {
      f32x4 acc[2][4] = {};
      bf16x8 N[2][8], Wf[2][4];
      const unsigned nb0 = (lane0 >> 4) * 1024 + (lane0 & 15) * 16 + 16384;
      const unsigned wa0 = (32 * w + (lane0 & 15)) * 128 + (((lane0 >> 4) ^ ((lane0 >> 1) & 7)) * 16);
      const unsigned nbA = opv(nb0), nbB = opv(nb0 + 2 * L_STAGE), waA = opv(wa0), waB = opv(wa0 + 2 * L_STAGE), wbA = opv(wa0 ^ 64), wbB = opv((wa0 ^ 64) + 2 * L_STAGE);
      auto rd = [&](int s) {
        const unsigned o = (s & 1) * L_STAGE;
        const unsigned nb = (s & 2) ? nbB : nbA, wa = (s & 2) ? waB : waA, wb = (s & 2) ? wbB : wbA;
        Wf[s & 1][0] = lds_frag(wa + o); Wf[s & 1][1] = lds_frag(wa + o + 2048);
#pragma unroll
        for (int i = 0; i < 4; ++i) N[s & 1][i] = lds_frag(nb + o + i * 256);
        Wf[s & 1][2] = lds_frag(wb + o); Wf[s & 1][3] = lds_frag(wb + o + 2048);
#pragma unroll
        for (int i = 0; i < 4; ++i) N[s & 1][4 + i] = lds_frag(nb + o + 4096 + i * 256);
      };
      auto mm = [&](int s) {
#pragma unroll
        for (int kk = 0; kk < 2; ++kk)
#pragma unroll
          for (int i = 0; i < 4; ++i) {
            acc[0][i] = MFMA16(N[s & 1][4 * kk + i], Wf[s & 1][2 * kk], acc[0][i]);
            acc[1][i] = MFMA16(N[s & 1][4 * kk + i], Wf[s & 1][2 * kk + 1], acc[1][i]);
          }
      };
      unsigned pk1 = 0;
      WAITV(13); BAR();
      STAMP(3);
      rd(0);
      WAITL0();
      WAITV(11);
      static_for<1, 32>([&](auto sc) {
        constexpr int s = decltype(sc)::value;
        SCHED(); BAR(); SCHED();
        rd(s);
        mm(s - 1);
#pragma unroll
        for (int i = 0; i < 12; ++i) { SGB(0x8, 1); SGB(0x100, 1); }
        SGB(0x8, 4);
        SCHED();
        WAITL0();
        SCHED();
        if constexpr (s == 1) {
          WAITV(9);
        } else if constexpr (s == 2) {
          WAITV(7);
        } else if constexpr (s == 3) {
          if (ops(b0) == 0 && w == 0 && lane0 == 0) flag_st(ws + WS_NONCE, epoch);
        } else if constexpr (s == H1) {
          const int b = ops(b0);
          const unsigned lane = opv(lane0);
          WAITV(0);
          LAS unsigned* cp = (LAS unsigned*)(L_HK + w * 256 + lane * 4);
          const unsigned cc = *cp;
          const bf16x8 f0 = lds_frag(L_LN + w * 2048 + lane * 16), f1 = lds_frag(L_LN + w * 2048 + 1024 + lane * 16);
          const float c0 = bflo(cc), c1 = bfhi(cc);
          *cp = pk2(c0 * __builtin_amdgcn_rcpf(1.f + __builtin_amdgcn_exp2f(c0 * -1.44269504f)), c1 * __builtin_amdgcn_rcpf(1.f + __builtin_amdgcn_exp2f(c1 * -1.44269504f)));
          if (b >= 128) {
            const unsigned so = WS_WDH + (unsigned)(((b & 7) * 16 + ((b >> 3) & 15)) * 8 + w) * 1024u;
            __builtin_amdgcn_raw_buffer_store_b128(__builtin_bit_cast(u32x4, f0), rws, lane * 16, so, 16);
            __builtin_amdgcn_raw_buffer_store_b128(__builtin_bit_cast(u32x4, f1), rws, lane * 16, so + 4096, 16);
          }
        } else if constexpr (s == H1 + 1) {
          const int b = ops(b0);
          const unsigned lane = opv(lane0), q = lane >> 4, r16 = lane & 15;
          f32x4 d = {}, d2 = {};
          const unsigned ma = L_WM + (8 * q + (r16 >> 2)) * 128 + (16 * w + 4 * (lane & 3)) * 2;
#pragma unroll
          for (int k = 0; k < 4; k += 2) {
            d = MFMA16(lds_frag(L_HK + w * 256 + (32 * k + 8 * q) * 2), lds_tr2(ma + k * 4096, ma + k * 4096 + 512), d);
            d2 = MFMA16(lds_frag(L_HK + w * 256 + (32 * k + 32 + 8 * q) * 2), lds_tr2(ma + k * 4096 + 4096, ma + k * 4096 + 4608), d2);
          }
          if (b < 192 && lane < 16) __builtin_amdgcn_raw_buffer_store_b32(f2u(d[0] + d2[0]), rws, lane * 4, WS_MODP + (unsigned)((b & 3) * 3072 + (b >> 2) * 64 + 16 * w) * 4u, 16);
          WAITL0();
        } else if constexpr (s == H1 + 4) {
          WAITV(0);
        } else if constexpr (s == H1 + 5) {
          if (w == 0) round_set(ws, 1, ops(b0), opv(lane0), epoch);
        } else if constexpr (s == H2) {
          const int b = ops(b0);
          const unsigned lane = opv(lane0);
          if (b < 224) {
            const unsigned g0 = (unsigned)(64 * (b / 28) + 16 * w + (lane >> 3)) * 3584u + (lane & 7) * 16;
            dma2n(mk_srd(p.wg, 512u * 1792u * 2u), g0, g0 + 8 * 3584 - 1024, (b % 28) * 128, L_AUX + w * 2048);
            dma2n(mk_srd(p.wu, 512u * 1792u * 2u), g0, g0 + 8 * 3584 - 1024, (b % 28) * 128, L_AUX + 8192 + w * 2048);
            const unsigned d0 = (unsigned)(64 * (b >> 3) + 16 * w + (lane >> 3)) * 1024u + (lane & 7) * 16;
            dma2n(mk_srd(p.wdn, 1792u * 512u * 2u), d0, d0 + 8 * 1024 - 1024, (b & 7) * 128, L_AUX + 16384 + w * 2048);
          }
          asm volatile("global_atomic_add %0, %1, %2, off sc0" : "=&v"(pk1) : "v"(round_word(ws, 1, b, w, lane)), "v"(0u) : "memory");
        } else if constexpr (s == H2 + 4) {
          asm volatile("s_waitcnt vmcnt(0)" : "+v"(pk1)::"memory");
          if (!all_lanes(pk1 == epoch)) {
            const char* fp = round_word(ws, 1, ops(b0), w, opv(lane0));
_Pragma("unroll 1") for (int sp = 0; sp < SPIN_CAP; ++sp) {
              if (all_lanes(atomic_peek(fp) == epoch)) break;
              __builtin_amdgcn_s_sleep(8);
            }
          }
          CFENCE();
        } else if constexpr (s == H2 + 5 || s == H2 + 6) {
          const int b = ops(b0);
          const unsigned lane = opv(lane0), q = lane >> 4, r16 = lane & 15;
          if constexpr (s == H2 + 5) {
            const int dn = C_DN(b);
            const int cb0 = (w == 3 ? 32 : 8 * w) + dn;
#pragma unroll
            for (int k = 0; k < 4; ++k) {
              dma1c(sws, lane * 16, WS_MODP + (unsigned)(k * 3072 + 1536 + 256 * w) * 4u, L_WM + w * 4096 + k * 1024);
              dma1w(sws, lane * 4, WS_MODP + (unsigned)(k * 3072 + cb0 * 64) * 4u, L_LN + w * 2048 + k * 256);
              dma1w(sws, (lane >> 5) * 16384 + (lane & 31) * 4, WS_RSP + (unsigned)((4 * k + C_PART(b)) * 2048 + 128 * C_TP(b) + 32 * w) * 4u, L_LN + w * 2048 + 1024 + k * 256);
            }
            dma1w(sws, lane * 4, WS_MODP + (unsigned)(w * 3072 + (40 + dn) * 64) * 4u, L_HK + w * 256);
          }
          if (b < 224) {
            const unsigned ga = L_AUX + (8 * q + (r16 >> 2)) * 128 + (16 * w + 4 * (lane & 3)) * 2;
#pragma unroll
            for (int k = 2 * (s - H2 - 5); k < 2 * (s - H2 - 5) + 2; ++k) {
              const unsigned a = ga + (k >> 1) * 4096 + (k & 1) * 8192;
              __builtin_amdgcn_raw_buffer_store_b128(__builtin_bit_cast(u32x4, lds_tr2(a, a + 512)), rws, lane * 16 + k * 1024, WS_WT + (unsigned)((4 * (b % 28) + w) * 32 + 4 * (b / 28)) * 1024u, 16);
            }
            const unsigned a = ga + 16384 + (s - H2 - 5) * 4096;
            __builtin_amdgcn_raw_buffer_store_b128(__builtin_bit_cast(u32x4, lds_tr2(a, a + 512)), rws, lane * 16, WS_WDT + (unsigned)(((b & 7) * 28 + (b >> 3)) * 8 + 4 * (s - H2 - 5) + w) * 1024u, 16);
          }
        } else if constexpr (s == H2 + 8) {
          const unsigned lane = opv(lane0);
          if (ops(b0) < 224) WAITV(6); else WAITV(0);
          const unsigned va = L_WM + w * 4096 + lane * 16;
          const f32x4 v = (lds_f4(va) + lds_f4(va + 1024)) + (lds_f4(va + 2048) + lds_f4(va + 3072));
          const LAS float* mp = (const LAS float*)(L_LN + w * 2048 + lane * 4);
          float m0 = rbf((mp[0] + mp[64]) + (mp[128] + mp[192]));
          const float rs = sum32((mp[256] + mp[320]) + (mp[384] + mp[448]));
          const float one = w >= 2 ? 1.f : 0.f;
          const u32x2 vb = {pk2(rbf(v[0]) + one, rbf(v[1]) + one), pk2(rbf(v[2]) + one, rbf(v[3]) + one)};
          *(LAS u32x2*)(L_V + w * 512 + lane * 8) = vb;
          if (w & 1) m0 = rbf(1.f + m0);
          if (w < 2 && C_PART(ops(b0))) m0 = -m0;
          *(LAS float*)(L_MOD + w * 256 + lane * 4) = m0;
          if (lane < 32) *(LAS float*)(L_RS + (32 * w + lane) * 4) = rs;
          WAITL0();
        } else if constexpr (s == H2 + 9) {
          if (w == 0) {
            const LAS float* mp = (const LAS float*)(L_HK + opv(lane0) * 4);
            *(LAS float*)(L_MOD + 1024 + opv(lane0) * 4) = rbf((mp[0] + mp[64]) + (mp[128] + mp[192]));
            WAITL0();
          }
        } else if constexpr (s == H2 + 10) {
          WAITV(0);
        } else if constexpr (s == H2 + 11) {
          if (w == 0) round_set(ws, 2, ops(b0), opv(lane0), epoch);
        } else if constexpr (s == 31) {
          const int b = ops(b0);
          const unsigned lane = opv(lane0);
          const __amdgpu_buffer_rsrc_t rx = mk_rsrc(p.x, 2048u * 512u * 2u);
#pragma unroll
          for (int e = 0; e < 4; ++e)
            xr[e] = __builtin_amdgcn_raw_buffer_load_b64(rx, (lane & 15) * 1024 + (lane >> 4) * 8 + e * 32, (unsigned)(64 * C_TB64(b) + 16 * w) * 1024u + C_DN(b) * 128, 0);
        }
      });
      const unsigned lane = opv(lane0), q = lane >> 4, r16 = lane & 15;
      const unsigned tb = opv(L_MOD + q * 16);
      f32x4 sa[4], c1[4];
#pragma unroll
      for (int i = 0; i < 4; ++i) { sa[i] = lds_f4(tb + i * 64); c1[i] = lds_f4(tb + 256 + i * 64); }
      const float rs0 = *(const LAS float*)(L_RS + (32 * w + r16) * 4), rs1 = *(const LAS float*)(L_RS + (32 * w + 16 + r16) * 4);
      SCHED();
      mm(31);
      SCHED();
      STAMP(4);
      const int b = ops(b0);
      const unsigned vo = ((q & 1) * 2 + (q >> 1)) * 256 + r16 * 16;
      const unsigned so = WS_PQ + (unsigned)((8 * C_TP(b) + 2 * w) * 32 + C_PART(b) * 16 + 2 * C_DN(b)) * 1024u;
#pragma unroll
      for (int tt = 0; tt < 2; ++tt) {
        const float rs = tt ? rs1 : rs0;
        unsigned pk[8];
#pragma unroll
        for (int i = 0; i < 4; ++i) {
          pk[2 * i] = pk2(fmaf(acc[tt][i][0], c1[i][0], rs * sa[i][0]), fmaf(acc[tt][i][1], c1[i][1], rs * sa[i][1]));
          pk[2 * i + 1] = pk2(fmaf(acc[tt][i][2], c1[i][2], rs * sa[i][2]), fmaf(acc[tt][i][3], c1[i][3], rs * sa[i][3]));
        }
#pragma unroll
        for (int pr = 0; pr < 2; ++pr)
          __builtin_amdgcn_raw_buffer_store_b128(merge16(pk[4 * pr], pk[4 * pr + 1], pk[4 * pr + 2], pk[4 * pr + 3]), rws, vo + pr * 1024, so + tt * 32768, 16);
      }
      WAITV(0);
      if (lane == 0) cnt_add(ws + WS_CPQ + (4 * C_TP(b) + w) * 256);
      STAMP(5);
    }

    float x1[16];
    {
      const int b = ops(b0);
      const unsigned lane = opv(lane0), q = lane >> 4, r16 = lane & 15, vl = lane * 16;
      const int tb64 = C_TB64(b), et = C_DN(b), tb16 = 4 * tb64 + w, t32c = 2 * tb64 + (w >> 1);
      lds_wait4(SY_WD);
      STAMP(15);
      cnt_wait(ws + WS_CPQ + t32c * 256, basePQ, lane, 1);
      STAMP(6);
      const int kh = w & 1;
      bf16x8 pq[32];
#pragma unroll
      for (int n = 0; n < 32; ++n)
        pq[n] = __builtin_bit_cast(bf16x8, __builtin_amdgcn_raw_buffer_load_b128(rws, vl + ((n >> 1) & 1) * 1024, WS_PQ + (unsigned)((2 * t32c + (n & 1)) * 32 + 16 * kh + (((n >> 2) + et) & 7) * 2) * 1024u, 16));
      SCHED();
      f32x4 y2[2][4] = {};
      bf16x8 F[64];
      unsigned fa;
      auto rdw = [&](int i) {
        if ((i & 7) == 0) {
          int nl = ((((i >> 3) + et) & 7) + 8 * kh - 4 * (tb64 & 3)) & 15;
          nl = nl >= 7 ? nl - 7 : nl + 12;
          fa = opv(vl + (unsigned)nl * 8192u);
        }
        F[i] = lds_frag(fa + (i & 7) * 1024);
      };
      static_for<0, 6>([&](auto ic) { rdw(decltype(ic)::value); });
      SCHED();
      static_for<0, 64>([&](auto ic) {
        constexpr int i = decltype(ic)::value;
        y2[0][i & 3] = MFMA16(F[i], pq[2 * (i >> 2)], y2[0][i & 3]);
        SCHED();
        y2[1][i & 3] = MFMA16(F[i], pq[2 * (i >> 2) + 1], y2[1][i & 3]);
        SCHED();
        if constexpr (i + 6 < 64) rdw(i + 6);
        if constexpr (i == PQ_SIG) { if (lane == 0) lds_inc(SY_PQL); }
        SCHED();
      });
      f32x4 y[4];
      pair_reduce(y, y2, 9 * 8192, SY_X3, w, lane);
      STAMP(7);
      const unsigned tb = opv(L_MOD + 512 + q * 16);
      f32x4 ga[4], c1[4];
#pragma unroll
      for (int e = 0; e < 4; ++e) { ga[e] = lds_f4(tb + e * 64); c1[e] = lds_f4(tb + 256 + e * 64); }
      unsigned pk[8];
      float sm = 0.f;
#pragma unroll
      for (int e = 0; e < 4; ++e) {
        x1[4 * e] = fmaf(ga[e][0], y[e][0], bflo(xr[e][0])); x1[4 * e + 1] = fmaf(ga[e][1], y[e][1], bfhi(xr[e][0]));
        x1[4 * e + 2] = fmaf(ga[e][2], y[e][2], bflo(xr[e][1])); x1[4 * e + 3] = fmaf(ga[e][3], y[e][3], bfhi(xr[e][1]));
        pk[2 * e] = pk2(x1[4 * e] * c1[e][0], x1[4 * e + 1] * c1[e][1]); pk[2 * e + 1] = pk2(x1[4 * e + 2] * c1[e][2], x1[4 * e + 3] * c1[e][3]);
        sm += (x1[4 * e] + x1[4 * e + 1]) + (x1[4 * e + 2] + x1[4 * e + 3]);
      }
      const unsigned vo = ((q & 1) * 2 + (q >> 1)) * 256 + r16 * 16;
#pragma unroll
      for (int pr = 0; pr < 2; ++pr)
        __builtin_amdgcn_raw_buffer_store_b128(merge16(pk[4 * pr], pk[4 * pr + 1], pk[4 * pr + 2], pk[4 * pr + 3]), rws, vo + pr * 1024, WS_AP + (unsigned)(tb16 * 16 + 2 * et) * 1024u, 16);
      const float mu = sum32(sum16(sm)) * (1.f / 64.f);
      float m2 = 0.f;
#pragma unroll
      for (int i = 0; i < 16; ++i) { const float d = x1[i] - mu; m2 = fmaf(d, d, m2); }
      m2 = sum32(sum16(m2));
      if (lane < 16) {
        const u32x2 o = {f2u(mu), f2u(m2)};
        __builtin_amdgcn_raw_buffer_store_b64(o, rws, lane * 8, WS_STATS + (unsigned)(et * 2048 + 16 * tb16) * 8u, 16);
      }
      if (lane == 0) lds_inc(SY_K3D);
      WAITV(0);
      if (lane == 0) cnt_add(ws + WS_CAP + t32c * 256);
      STAMP(8);
    }
    __builtin_amdgcn_s_setprio(0);
    {
      const int b = ops(b0);
      const unsigned lane = opv(lane0);
      bf16x8 W[32];
      lds_wait_ge(SY_AUX, 1);
      load_w(W, rws, 7 * C_FT(b) + w, C_LOC(b) >> 2, lane);
      k4_body(W, rws, ws, w, lane, C_TP4(b), C_FT(b), C_LOC(b) >> 2);
    }
    STAMP(9);

    {
      const int b = ops(b0);
      const unsigned lane = opv(lane0), q = lane >> 4, r16 = lane & 15, vl = lane * 16;
      const int tb64 = C_TB64(b), et = C_DN(b), tb16 = 4 * tb64 + w, t32c = 2 * tb64 + (w >> 1);
      cnt_wait(ws + WS_CG + t32c * 256, baseG, lane, 1);
      STAMP(10);
      const int kh = w & 1, rot5 = C_ROT5(b);
      const unsigned gb0 = WS_G + (unsigned)(2 * t32c) * 57344u + (unsigned)kh * 28672u;
      const unsigned vy = opv(vl + 57344);
      bf16x8 g[56];
      auto ldg = [&](int m) {
        int kb = m + rot5; kb = kb >= 14 ? kb - 14 : kb;
#pragma unroll
        for (int i = 0; i < 4; ++i)
          g[4 * m + i] = __builtin_bit_cast(bf16x8, __builtin_amdgcn_raw_buffer_load_b128(rws, ((i & 1) ? vy : vl) + (i >> 1) * 1024, gb0 + (unsigned)kb * 2048u, 16));
      };
      static_for<0, G_WIN>([&](auto mc) { ldg(decltype(mc)::value); });
      f32x4 y2[2][4] = {};
      bf16x8 F[112];
      unsigned fa;
      auto rdw = [&](int i) {
        if ((i & 7) == 0) {
          const int m = i >> 3;
          if (m == 5) lds_wait_ge(SY_WL + 1, 1);
          if (m == 9) lds_wait_ge(SY_WL + 2, 1);
          if (m == 13) lds_wait_ge(SY_WL + 3, 1);
          const int s0 = (16 + 2 * m) % 19, s1 = (17 + 2 * m) % 19;
          fa = opv(vl + (unsigned)(kh ? s1 : s0) * 8192u);
        }
        F[i] = lds_frag(fa + (i & 7) * 1024);
      };
      lds_wait_ge(SY_WL + 0, 1);
      static_for<0, 6>([&](auto ic) { rdw(decltype(ic)::value); });
      SCHED();
      static_for<0, 112>([&](auto ic) {
        constexpr int i = decltype(ic)::value;
        y2[0][i & 3] = MFMA16(F[i], g[2 * (i >> 2)], y2[0][i & 3]);
        SCHED();
        y2[1][i & 3] = MFMA16(F[i], g[2 * (i >> 2) + 1], y2[1][i & 3]);
        SCHED();
        if constexpr (i + 6 < 112) rdw(i + 6);
        if constexpr ((i & 7) == 7 && (i >> 3) + G_WIN < 14) ldg((i >> 3) + G_WIN);
        if constexpr (i == 31 || i == 47) { if (lane == 0) lds_inc(SY_PROG + (i == 47)); }
        if constexpr (i == 31 || i == 55 || i == 87) STAMP(24 + (i >> 5) + (i == 55));
        SCHED();
      });
      lds_wait_ge(SY_PROG + 1, 4);
      f32x4 y[4];
      pair_reduce(y, y2, 6 * 8192, SY_X5, w, lane);
      STAMP(11);
      const unsigned tb = opv(L_MOD + 1024 + q * 16);
      unsigned pk[8];
#pragma unroll
      for (int e = 0; e < 4; ++e) {
        const f32x4 gb = lds_f4(tb + e * 64);
        pk[2 * e] = pk2(fmaf(gb[0], y[e][0], x1[4 * e]), fmaf(gb[1], y[e][1], x1[4 * e + 1]));
        pk[2 * e + 1] = pk2(fmaf(gb[2], y[e][2], x1[4 * e + 2]), fmaf(gb[3], y[e][3], x1[4 * e + 3]));
      }
      const __amdgpu_buffer_rsrc_t ro = mk_rsrc(p.out, 2048u * 512u * 2u);
#pragma unroll
      for (int pr = 0; pr < 2; ++pr)
        __builtin_amdgcn_raw_buffer_store_b128(merge16(pk[4 * pr], pk[4 * pr + 1], pk[4 * pr + 2], pk[4 * pr + 3]), ro, r16 * 1024 + (q & 1) * 32 + (q >> 1) * 16 + pr * 64,
                                               (unsigned)(16 * tb16) * 1024u + et * 128, 0);
    }
    STAMP(12);
    if (wv == 0 && lane0 == 0) ((unsigned*)(ws + WS_DIAG))[64 + b0] = (unsigned)__builtin_amdgcn_s_memrealtime();
  }
}

#if defined(__HIP_DEVICE_COMPILE__)
#pragma clang attribute pop
#endif

__global__ void tinyA(unsigned* p) { if (threadIdx.x == 0) p[0] = (unsigned)__builtin_amdgcn_s_memrealtime(); }
__global__ void tinyC(unsigned* p, int spin) {
  const unsigned t = (unsigned)__builtin_amdgcn_s_memrealtime();
  unsigned d = t - p[64 + threadIdx.x];
  __shared__ unsigned sm[256];
  sm[threadIdx.x] = d; __syncthreads();
  if (threadIdx.x == 0 && spin) { for (int i = 0; i < 256; ++i) d = sm[i] < d ? sm[i] : d; d = d > 3000 ? 3000 : d; while ((unsigned)__builtin_amdgcn_s_memrealtime() - t < 8u * d) __builtin_amdgcn_s_sleep(1); }
}
extern "C" void kernel_launch(void* const* d_in, const int* in_sizes, int n_in, void* d_out, int out_size, void* d_ws, size_t ws_size,
                              hipStream_t stream) {
  Params p;
  p.x = (const u16*)d_in[0]; p.wsre = (const u16*)d_in[1]; p.wsim = (const u16*)d_in[2];
  p.wdre = (const u16*)d_in[3]; p.wdim = (const u16*)d_in[4];
  p.wg = (const u16*)d_in[6]; p.wu = (const u16*)d_in[7]; p.wdn = (const u16*)d_in[8];
  p.c = (const u16*)d_in[9]; p.wmod = (const u16*)d_in[10];
  p.out = (u16*)d_out; p.ws = (char*)d_ws;
  (void)hipFuncSetAttribute((const void*)fused_block, hipFuncAttributeMaxDynamicSharedMemorySize, LDS_BYTES);
  fused_block<<<256, 512, LDS_BYTES, stream>>>(p);
  tinyC<<<1, 256, 0, stream>>>((unsigned*)(p.ws + WS_DIAG), XC == 2);
}
```

```cpp
#include <hip/hip_runtime.h>

typedef unsigned short u16;
typedef unsigned long long u64;
typedef short bf16x8 __attribute__((ext_vector_type(8)));
typedef short s16x4 __attribute__((ext_vector_type(4)));
typedef __bf16 bf2 __attribute__((ext_vector_type(2)));
typedef float f32x16 __attribute__((ext_vector_type(16)));
typedef float f32x4 __attribute__((ext_vector_type(4)));
typedef float f32x2 __attribute__((ext_vector_type(2)));
typedef unsigned u32x4 __attribute__((ext_vector_type(4)));
typedef unsigned u32x2 __attribute__((ext_vector_type(2)));
#define DI __device__ __forceinline__
#define LAS __attribute__((address_space(3)))
#define GAS __attribute__((address_space(1)))

constexpr unsigned WS_NONCE = 0;
constexpr unsigned WS_FLAG = 131072;
constexpr unsigned WS_CPQ = 16384;
constexpr unsigned WS_CAP = 32768;
constexpr unsigned WS_CG = 49152;
constexpr unsigned WS_MODP = 65536;
constexpr unsigned WS_STATS = 393216;
constexpr unsigned WS_RSP = 524288;
constexpr unsigned WS_NT = 1u << 20;
constexpr unsigned WS_PQ = 4u << 20;
constexpr unsigned WS_AP = 8u << 20;
constexpr unsigned WS_G = 10u << 20;
constexpr unsigned WS_WT = 18u << 20;
constexpr unsigned WS_WDT = 22u << 20;
constexpr unsigned WS_WDH = 24u << 20;
constexpr unsigned WS_DIAG = 25u << 20;
constexpr unsigned WS_BYTES = 26u << 20;

constexpr unsigned L_STAGE = 24576;
constexpr unsigned L_AUX = 98304;
constexpr unsigned L_LN = 122880;
constexpr unsigned L_WM = 131840;
constexpr unsigned L_HK = 148224;
constexpr unsigned L_TAB = 155648;
constexpr unsigned L_MOD = L_TAB;
constexpr unsigned L_V = L_TAB + 1280;
constexpr unsigned L_ST = L_TAB + 3328;
constexpr unsigned L_SY = L_TAB + 4352;
constexpr unsigned LDS_BYTES = 163840;
enum { SY_ADONE = 0, SY_STAT = 4, SY_UFIN = 5, SY_GSIG = 9, SY_WL = 10, SY_PROG = 14, SY_LN = 16, SY_ND = 17, SY_AUX = 18, SY_PQL = 19, SY_WD = 20, SY_X3 = 24, SY_X5 = 28, SY_WIS = 32, SY_K3D = 33, SY_N = 34, SY_EP = 35, SY_BAP = 36 };
constexpr unsigned L_RS = L_TAB + 6400;
#define PRE_H 4
#define L_PACE 6
#define L_EARLY 26
#define G_WIN 4
#define W_SIG 21
#define H1 6
#define H2 16
#define PQ_SIG 40
#define W_PACE 4
#define WD_PACE 6

constexpr int SPIN_CAP = 1 << 20;
constexpr int CODE_LINES_PER_WG = 10;

struct Params {
  const u16 *x, *wsre, *wsim, *wdre, *wdim, *wg, *wu, *wdn, *c, *wmod;
  u16* out;
  char* ws;
};

#if defined(__HIP_DEVICE_COMPILE__)
#pragma clang attribute push(__attribute__((target("no-packed-fp32-ops"))), apply_to = function)
#endif
DI float u2f(unsigned u) { return __builtin_bit_cast(float, u); }
DI unsigned f2u(float f) { return __builtin_bit_cast(unsigned, f); }
DI bool all_lanes(bool c) { return __builtin_amdgcn_ballot_w64(c) == ~0ull; }
DI u32x4 mk_srd(const void* p, unsigned bytes) {
  u64 a = (u64)p;
  u32x4 r = {(unsigned)a, (unsigned)(a >> 32) & 0xffffu, bytes, 0x00020000u};
  return r;
}
DI __amdgpu_buffer_rsrc_t mk_rsrc(const void* p, unsigned bytes) {
  return __builtin_amdgcn_make_buffer_rsrc((void*)p, 0, bytes, 0x00020000);
}
#define RFL(x) __builtin_amdgcn_readfirstlane(x)
#define BAR() __builtin_amdgcn_s_barrier()
#define WAITV(n) asm volatile("s_waitcnt vmcnt(%0)" ::"i"(n) : "memory")
#define WAITL0() __builtin_amdgcn_s_waitcnt(0xC07F)
#define CFENCE() asm volatile("" ::: "memory")

DI void dma4v(u32x4 srd, unsigned v0, unsigned v1, unsigned v2, unsigned v3, unsigned soff, unsigned m0v) {
  asm volatile("s_mov_b32 m0, %0\n\ts_nop 0\n\t"
               "buffer_load_dwordx4 %1, %5, %6 offen lds\n\t"
               "buffer_load_dwordx4 %2, %5, %6 offen offset:1024 lds\n\t"
               "buffer_load_dwordx4 %3, %5, %6 offen offset:2048 lds\n\t"
               "buffer_load_dwordx4 %4, %5, %6 offen offset:3072 lds"
               ::"s"(m0v), "v"(v0), "v"(v1), "v"(v2), "v"(v3), "s"(srd), "s"(soff) : "memory");
}
DI void dma2v(u32x4 srd, unsigned v0, unsigned v1, unsigned soff, unsigned m0v) {
  asm volatile("s_mov_b32 m0, %0\n\ts_nop 0\n\t"
               "buffer_load_dwordx4 %1, %3, %4 offen lds\n\t"
               "buffer_load_dwordx4 %2, %3, %4 offen offset:1024 lds"
               ::"s"(m0v), "v"(v0), "v"(v1), "s"(srd), "s"(soff) : "memory");
}
DI void dma2n(u32x4 srd, unsigned v0, unsigned v1, unsigned soff, unsigned m0v) {
  asm volatile("s_mov_b32 m0, %0\n\ts_nop 0\n\t"
               "buffer_load_dwordx4 %1, %3, %4 offen nt lds\n\t"
               "buffer_load_dwordx4 %2, %3, %4 offen offset:1024 nt lds"
               ::"s"(m0v), "v"(v0), "v"(v1), "s"(srd), "s"(soff) : "memory");
}
DI void dma2c(u32x4 srd, unsigned v, unsigned soff, unsigned m0v) {
  asm volatile("s_mov_b32 m0, %0\n\ts_nop 0\n\t"
               "buffer_load_dwordx4 %1, %2, %3 offen sc1 lds\n\t"
               "buffer_load_dwordx4 %1, %2, %3 offen offset:1024 sc1 lds"
               ::"s"(m0v), "v"(v), "s"(srd), "s"(soff) : "memory");
}
DI void dma4c(u32x4 srd, unsigned v, unsigned soff, unsigned m0v) {
  asm volatile("s_mov_b32 m0, %0\n\ts_nop 0\n\t"
               "buffer_load_dwordx4 %1, %2, %3 offen sc1 lds\n\t"
               "buffer_load_dwordx4 %1, %2, %3 offen offset:1024 sc1 lds\n\t"
               "buffer_load_dwordx4 %1, %2, %3 offen offset:2048 sc1 lds\n\t"
               "buffer_load_dwordx4 %1, %2, %3 offen offset:3072 sc1 lds"
               ::"s"(m0v), "v"(v), "s"(srd), "s"(soff) : "memory");
}
DI void dma1w(u32x4 srd, unsigned v, unsigned soff, unsigned m0v) {
  asm volatile("s_mov_b32 m0, %0\n\ts_nop 0\n\tbuffer_load_dword %1, %2, %3 offen sc1 lds" ::"s"(m0v), "v"(v), "s"(srd), "s"(soff) : "memory");
}
DI void dma1c(u32x4 srd, unsigned v, unsigned soff, unsigned m0v) {
  asm volatile("s_mov_b32 m0, %0\n\ts_nop 0\n\tbuffer_load_dwordx4 %1, %2, %3 offen sc1 lds" ::"s"(m0v), "v"(v), "s"(srd), "s"(soff) : "memory");
}
DI unsigned opv(unsigned v) { asm volatile("" : "+v"(v)); return v; }
DI int ops(int v) { asm volatile("" : "+s"(v)); return v; }
#define SCHED() __builtin_amdgcn_sched_barrier(0)
#define SGB(m, n) __builtin_amdgcn_sched_group_barrier((m), (n), 0)
DI bf16x8 lds_frag(unsigned a) { return *(const LAS bf16x8*)a; }
DI f32x4 lds_f4(unsigned a) { return *(const LAS f32x4*)a; }
DI bf16x8 lds_tr2(unsigned a0, unsigned a1) {
  s16x4 lo = __builtin_amdgcn_ds_read_tr16_b64_v4i16((LAS s16x4*)a0);
  s16x4 hi = __builtin_amdgcn_ds_read_tr16_b64_v4i16((LAS s16x4*)a1);
  return __builtin_shufflevector(lo, hi, 0, 1, 2, 3, 4, 5, 6, 7);
}
DI unsigned pk2(float a, float b) { bf2 p = {(__bf16)a, (__bf16)b}; return __builtin_bit_cast(unsigned, p); }
DI float bflo(unsigned u) { return u2f(u << 16); }
DI float bfhi(unsigned u) { return u2f(u & 0xffff0000u); }
DI float rbf(float a) { return (float)(__bf16)a; }
DI float sum32(float v) { auto r = __builtin_amdgcn_permlane32_swap(f2u(v), f2u(v), false, false); return u2f(r[0]) + u2f(r[1]); }
DI float sum16(float v) { auto r = __builtin_amdgcn_permlane16_swap(f2u(v), f2u(v), false, false); return u2f(r[0]) + u2f(r[1]); }
template <int CTRL> DI float dpp_add(float v) { return v + u2f(__builtin_amdgcn_update_dpp(0u, f2u(v), CTRL, 0xf, 0xf, true)); }
DI float wave_sum(float v) {
  v = dpp_add<0xB1>(v);
  v = dpp_add<0x4E>(v);
  v = dpp_add<0x141>(v);
  v = dpp_add<0x140>(v);
  return sum32(sum16(v));
}
#define MFMA16(a, b, c) __builtin_amdgcn_mfma_f32_16x16x32_bf16((a), (b), (c), 0, 0, 0)

DI unsigned atomic_peek(const char* p) {
  unsigned r, z = 0;
  asm volatile("global_atomic_add %0, %1, %2, off sc0\n\ts_waitcnt vmcnt(0)" : "=&v"(r) : "v"(p), "v"(z) : "memory");
  return r;
}
DI void cnt_add(char* p) { asm volatile("global_atomic_add %0, %1, off" ::"v"(p), "v"(1u) : "memory"); }
DI unsigned flag_ld(const char* p) { return __hip_atomic_load((GAS unsigned*)p, __ATOMIC_RELAXED, __HIP_MEMORY_SCOPE_AGENT); }
DI void flag_st(char* p, unsigned v) { asm volatile("global_atomic_swap %0, %1, off" ::"v"(p), "v"(v) : "memory"); }
DI void round_set(char* ws, int round, int b, unsigned lane, unsigned epoch) {
  if (lane < 16) flag_st(ws + WS_FLAG + round * 16384 + lane * 1024 + (((b >> 1) & 3) * 64 + (b >> 3) * 2 + (b & 1)) * 4, epoch);
}
DI const char* round_word(const char* ws, int round, int b, int quarter, unsigned lane) { return ws + WS_FLAG + round * 16384 + ((b >> 3) & 15) * 1024 + (quarter * 64 + lane) * 4; }
DI void cnt_wait(const char* p, unsigned base, int lane, int nl) {
_Pragma("unroll 1") for (int sp = 0; sp < SPIN_CAP; ++sp) {
    unsigned v = base + 16u;
    if (lane < nl) v = atomic_peek(p);
    if (all_lanes(v - base >= 16u)) break;
  }
  CFENCE();
}
DI void lds_wait_ge(unsigned word, unsigned want) {
  volatile LAS unsigned* p = (volatile LAS unsigned*)(L_SY + word * 4);
_Pragma("unroll 1") for (int sp = 0; sp < SPIN_CAP; ++sp) {
    if (RFL(*p) >= want) break;
    __builtin_amdgcn_s_sleep(1);
  }
  CFENCE();
}
DI void lds_wait4(unsigned word) {
  volatile LAS u32x4* p = (volatile LAS u32x4*)(L_SY + word * 4);
_Pragma("unroll 1") for (int sp = 0; sp < SPIN_CAP; ++sp) {
    const u32x4 v = *p;
    if (RFL(v[0] & v[1] & v[2] & v[3])) break;
    __builtin_amdgcn_s_sleep(1);
  }
  CFENCE();
}
DI unsigned lds_get(unsigned word) { return *(volatile LAS unsigned*)(L_SY + word * 4); }
DI void lds_set(unsigned word, unsigned v) { *(volatile LAS unsigned*)(L_SY + word * 4) = v; }
DI unsigned lds_inc(unsigned word) { return __hip_atomic_fetch_add((LAS unsigned*)(L_SY + word * 4), 1u, __ATOMIC_RELAXED, __HIP_MEMORY_SCOPE_WORKGROUP); }

#define STAMP(i)
#define STAMPB(i)

template <int I> struct IC { static constexpr int value = I; };
template <int A, int B, class F> DI void static_for(F&& f) { if constexpr (A < B) { f(IC<A>{}); static_for<A + 1, B>(f); } }

DI u32x4 merge16(unsigned a0, unsigned a1, unsigned b0, unsigned b1) {
  auto s0 = __builtin_amdgcn_permlane16_swap(a0, b0, false, false);
  auto s1 = __builtin_amdgcn_permlane16_swap(a1, b1, false, false);
  const u32x4 o = {s0[0], s1[0], s0[1], s1[1]};
  return o;
}

DI void pair_reduce(f32x4 (&y)[4], const f32x4 (&y2)[2][4], unsigned scratch, unsigned word, int w, unsigned lane) {
  const bool odd = w & 1;
  const unsigned a = scratch + lane * 16;
#pragma unroll
  for (int e = 0; e < 4; ++e) *(LAS f32x4*)(a + w * 4096 + e * 1024) = odd ? y2[0][e] : y2[1][e];
  CFENCE();
  lds_set(word + w, 1);
  lds_wait_ge(word + (w ^ 1), 1);
#pragma unroll
  for (int e = 0; e < 4; ++e) y[e] = (odd ? y2[1][e] : y2[0][e]) + lds_f4(a + (w ^ 1) * 4096 + e * 1024);
}

DI void load_w(bf16x8 (&W)[32], __amdgpu_buffer_rsrc_t rws, int c, int rot, unsigned lane) {
  const unsigned vl = opv(lane * 16);
#pragma unroll
  for (int k = 0; k < 32; ++k) {
    W[k] = __builtin_bit_cast(bf16x8, __builtin_amdgcn_raw_buffer_load_b128(rws, vl + (k & 3) * 1024, WS_WT + (unsigned)(c * 32 + (((k >> 2) + rot) & 7) * 4) * 1024u, 16));
    if ((k & 1) == 1 && k >= W_PACE && k < 31) WAITV(W_PACE);
    if (k == W_SIG && lane == 0) lds_inc(SY_WIS);
  }
  SCHED();
}
template <int N> DI float row_bcast(float v) { return u2f(__builtin_amdgcn_update_dpp(0u, f2u(v), 0x150 + N, 0xf, 0xf, true)); }

DI float geglu(float g, float u, float rstd, float nmr, float ug, float vg, float uu, float vu) {
  const float a = fmaf(g, rstd, fmaf(nmr, ug, vg));
  const float b = fmaf(u, rstd, fmaf(nmr, uu, vu));
  const float e = a * fmaf(a * a, -0.10294324f, -2.30220820f);
  return a * b * __builtin_amdgcn_rcpf(1.f + __builtin_amdgcn_exp2f(e));
}

DI void k4_body(const bf16x8 (&W)[32], __amdgpu_buffer_rsrc_t rws, char* ws, int wv, unsigned lane, int tp4, int ft, int rot) {
  const unsigned q = lane >> 4, r16 = lane & 15;
  const int c = 7 * ft + wv;
  f32x4 ug, vg, uu, vu;
  {
    f32x4 dg = {}, du = {};
    const unsigned va = L_V + (lane & 1) * 1024 + q * 16;
#pragma unroll
    for (int k = 0; k < 16; ++k) {
      const bf16x8 f = lds_frag(va + (unsigned)((((k >> 1) + rot) & 7) * 128 + (k & 1) * 64));
      dg = MFMA16(W[2 * k], f, dg); du = MFMA16(W[2 * k + 1], f, du);
    }
#pragma unroll
    for (int j = 0; j < 4; ++j) { vg[j] = row_bcast<0>(dg[j]); ug[j] = row_bcast<1>(dg[j]); vu[j] = row_bcast<0>(du[j]); uu[j] = row_bcast<1>(du[j]); }
  }
  SCHED();
  const unsigned gvoff = (q & 1) * 57344 + (c >> 1) * 1024 + (c & 1) * 512 + (q >> 1) * 256 + r16 * 16;
  const unsigned sb = opv(L_ST + r16 * 8);
  unsigned ab[8];
#pragma unroll
  for (int g = 0; g < 8; ++g) ab[g] = lane * 16 + (unsigned)(((g + rot) & 7) * 2048);
  lds_wait_ge(SY_ADONE + 0, 1);
#pragma unroll 1
  for (int u = 0; u < 4; ++u) {
    const int tbl = (u + ft) & 3;
    STAMP(16 + 2 * u);
    f32x4 gx = {}, ux = {}, gy = {}, uy = {};
    bf16x8 A[32];
    auto rda = [&](int i) { A[i] = lds_frag(ab[i >> 2] + ((i >> 1) & 1) * 1024 + (i & 1) * 16384); };
    static_for<0, 6>([&](auto ic) { rda(decltype(ic)::value); });
    unsigned nxt, sto;
    SCHED();
    static_for<0, 32>([&](auto ic) {
      constexpr int i = decltype(ic)::value;
      if constexpr (i & 1) { gy = MFMA16(W[i - 1], A[i], gy); SCHED(); uy = MFMA16(W[i], A[i], uy); }
      else { gx = MFMA16(W[i], A[i], gx); SCHED(); ux = MFMA16(W[i + 1], A[i], ux); }
      SCHED();
      if constexpr (i + 6 < 32) rda(i + 6);
      if constexpr (i == 26) nxt = lds_get(SY_ADONE + u + 1);
      if constexpr (i == 27) sto = lds_get(SY_STAT);
      SCHED();
    });
    STAMP(17 + 2 * u);
    if (lane == 0) lds_inc(SY_UFIN + u);
    if (!RFL(sto)) lds_wait_ge(SY_STAT, 1);
    const f32x2 sx = *(const LAS f32x2*)(sb + tbl * 256), sy = *(const LAS f32x2*)(sb + tbl * 256 + 128);
#pragma unroll
    for (int g = 0; g < 8; ++g) ab[g] += 32768;
    float ox[4], oy[4];
#pragma unroll
    for (int j = 0; j < 4; ++j) {
      ox[j] = geglu(gx[j], ux[j], sx[0], sx[1], ug[j], vg[j], uu[j], vu[j]);
      oy[j] = geglu(gy[j], uy[j], sy[0], sy[1], ug[j], vg[j], uu[j], vu[j]);
    }
    __builtin_amdgcn_raw_buffer_store_b128(merge16(pk2(ox[0], ox[1]), pk2(ox[2], ox[3]), pk2(oy[0], oy[1]), pk2(oy[2], oy[3])), rws, gvoff,
                                           WS_G + (unsigned)(2 * (4 * tp4 + tbl)) * 57344u, 16);
    if (!RFL(nxt)) lds_wait_ge(SY_ADONE + u + 1, 1);
  }
  WAITV(0);
  unsigned old = 0;
  if (lane == 0) old = lds_inc(SY_GSIG);
  if (RFL(old) == 6 && lane < 4) cnt_add(ws + WS_CG + (4 * tp4 + lane) * 256);
}

#define C_XCD(b) ((b) & 7)
#define C_LOC(b) ((b) >> 3)
#define C_TP(b) (2 * C_XCD(b) + (C_LOC(b) >> 4))
#define C_PART(b) ((C_LOC(b) >> 3) & 1)
#define C_DN(b) (C_LOC(b) & 7)
#define C_J0(b) (4 * ((C_DN(b) + C_XCD(b) + 4 * C_PART(b)) & 7))
#define C_TB64(b) (4 * C_XCD(b) + (C_LOC(b) >> 3))
#define C_TP4(b) (8 * (C_XCD(b) & 1) + (C_LOC(b) >> 2))
#define C_FT(b) (4 * (C_XCD(b) >> 1) + (C_LOC(b) & 3))
#define C_ROT5(b) ((7 * C_DN(b) / 4 + 7 * (C_TB64(b) & 3) / 2) % 14)

__global__ __launch_bounds__(512, 2) void fused_block(Params p) {
  const int b0 = blockIdx.x;
  const unsigned lane0 = threadIdx.x & 63;
  const int wv = RFL(threadIdx.x >> 6);
  char* const ws = p.ws;
  u64 pc0;
  asm volatile("s_getpc_b64 %0" : "=s"(pc0));
  STAMP(0);
  const u32x4 sws = mk_srd(ws, WS_BYTES);
  const __amdgpu_buffer_rsrc_t rws = mk_rsrc(ws, WS_BYTES);

  if (wv >= 4) {
    const int wl = wv - 4;
    const unsigned lane = lane0;
    const unsigned sw = (lane & 7) ^ (lane >> 4);
    const unsigned vl = lane * 16;
    {
      const int b = b0, j0 = C_J0(b), dn = C_DN(b);
      unsigned junk = 0;
      if (wv == 4 && lane < CODE_LINES_PER_WG)
        asm volatile("global_load_dword %0, %1, off" : "=&v"(junk) : "v"((const char*)(pc0 & ~255ull) + (C_LOC(b) * CODE_LINES_PER_WG + lane) * 128) : "memory");
      const u32x4 sA = mk_srd(C_PART(b) ? p.wsim : p.wsre, 2048u * 2048u * 2u);
      const unsigned ra = (unsigned)(128 * C_TP(b) + 32 * wl + (lane >> 3)) * 4096u;
      const unsigned va0 = ra + sw * 16, va1 = ra + 8 * 4096 + (sw ^ 4) * 16 - 1024, va2 = ra + 16 * 4096 + sw * 16 - 2048, va3 = ra + 24 * 4096 + (sw ^ 4) * 16 - 3072;
      const unsigned o1 = (lane & 11) == 0 ? 0x3f803f80u : 0u;
      const u32x4 ones4 = {o1, o1, o1, o1};
      const bf16x8 sel = __builtin_bit_cast(bf16x8, ones4);
      const unsigned wa = (32 * wl + (lane & 15)) * 128 + (((lane >> 4) ^ ((lane >> 1) & 7)) * 16);
      f32x4 r0 = {}, r1 = {};
      auto rsum = [&](int s) {
        const unsigned a = wa + s * L_STAGE;
        r0 = MFMA16(sel, lds_frag(a), r0); r1 = MFMA16(sel, lds_frag(a + 2048), r1);
        r0 = MFMA16(sel, lds_frag(a ^ 64), r0); r1 = MFMA16(sel, lds_frag((a ^ 64) + 2048), r1);
      };
      auto half = [&](int hs) {
        if (hs & 1) dma2n(sA, va2 + 2048, va3 + 2048, (unsigned)(j0 + (hs >> 1)) * 128u, (hs >> 1) * L_STAGE + wl * 4096 + 2048);
        else dma2n(sA, va0, va1, (unsigned)(j0 + (hs >> 1)) * 128u, (hs >> 1) * L_STAGE + wl * 4096);
      };
      half(0);
      BAR();
#pragma unroll
      for (int hs = 1; hs < PRE_H; ++hs) half(hs);
      STAMP(1);
      BAR();
      asm volatile("" : "+v"(junk));
#pragma unroll
      for (int hs = PRE_H; hs < 8; ++hs) half(hs);
      asm volatile("s_waitcnt vmcnt(12)" : "+v"(junk)::"memory");
      BAR();
      STAMP(2);
      rsum(0);
      WAITV(8); BAR();
      STAMPB(1);
      auto stage1 = [&](int s) {
        const unsigned j = (unsigned)(j0 + s) & 31u, sl = (unsigned)(s & 3) * L_STAGE;
        WAITV(L_PACE);
        dma2c(sws, vl, WS_NT + ((unsigned)dn * 32u + j) * 8192u + wl * 2048, sl + 16384 + wl * 2048);
      };
      auto stage2 = [&](int s) {
        const unsigned j = (unsigned)(j0 + s) & 31u, sl = (unsigned)(s & 3) * L_STAGE;
        WAITV(L_PACE);
        dma2v(sA, va0, va1, j * 128u, sl + wl * 4096);
        WAITV(L_PACE);
        dma2v(sA, va2 + 2048, va3 + 2048, j * 128u, sl + wl * 4096 + 2048);
      };
      auto stage = [&](int s) { stage1(s); stage2(s); };
      rsum(1);
      WAITL0();
      stage(4);
      WAITV(10); BAR();
      STAMPB(2);
      rsum(2);
      stage(5);
      WAITV(12); BAR();
      STAMPB(3);
      rsum(3);
      if (lane < 32) __builtin_amdgcn_raw_buffer_store_b32(f2u(lane < 16 ? r0[0] : r1[0]), rws, lane * 4, WS_RSP + (unsigned)((dn * 2 + C_PART(b)) * 2048 + 128 * C_TP(b) + 32 * wl) * 4u, 16);
      stage(6);
      WAITV(12); BAR();
      STAMPB(4);
#pragma unroll 1
      for (int s = 5; s < L_EARLY; ++s) { stage(s + 2); WAITV(12); BAR(); STAMPB(s); if ((s & 3) == 0) STAMP(23 + (s >> 2)); }
#pragma unroll 1
      for (int s = L_EARLY; s <= 29; ++s) { stage1(s + 2); WAITV(8); BAR(); STAMPB(s); if ((s & 3) == 0) STAMP(23 + (s >> 2)); stage2(s + 2); }
    }
    const int b = ops(b0);
    {
      auto wdblk = [&](int n) {
        const unsigned blk = (unsigned)(n + 4 * (C_TB64(b) & 3)) & 15u;
        dma2c(sws, vl, WS_WDH + ((unsigned)C_DN(b) * 16u + blk) * 8192u + wl * 2048, (unsigned)(n < 7 ? 12 + n : n - 7) * 8192u + wl * 2048);
      };
      WAITV(6); BAR();
      STAMPB(30);
      WAITV(4); wdblk(0); WAITV(4); wdblk(1);
      WAITV(4); BAR();
      STAMP(3);
#pragma unroll 1
      for (int n = 2; n < 16; ++n) { WAITV(WD_PACE); wdblk(n); if ((n & 3) == 0) STAMP(9 + (n >> 2)); }
    }
    const int tp4 = C_TP4(b), ft = C_FT(b), rot4 = C_LOC(b) >> 2;
    if (wv < 7) {
      bf16x8 W[32];
      WAITV(0);
      lds_set(SY_WD + wl, 1);
      STAMP(4);
      lds_wait_ge(SY_AUX, 1);
      lds_wait_ge(SY_PQL, 4);
      load_w(W, rws, 7 * ft + wv, rot4, lane);
      STAMP(5);
      k4_body(W, rws, ws, wv, lane, tp4, ft, rot4);
      STAMP(8);
    } else {
      WAITV(0);
      lds_set(SY_WD + 3, 1);
      STAMP(4);
      {
        const unsigned ep = RFL(lds_get(SY_EP));
        const char* fp = round_word(ws, 2, b, 0, lane);
_Pragma("unroll 1") for (int sp = 0; sp < SPIN_CAP; ++sp) {
          unsigned f0, f1, f2, f3;
          asm volatile("global_atomic_add %0, %4, %5, off sc0\n\tglobal_atomic_add %1, %4, %5, off offset:256 sc0\n\tglobal_atomic_add %2, %4, %5, off offset:512 sc0\n\t"
                       "global_atomic_add %3, %4, %5, off offset:768 sc0\n\ts_waitcnt vmcnt(0)"
                       : "=&v"(f0), "=&v"(f1), "=&v"(f2), "=&v"(f3) : "v"(fp), "v"(0u) : "memory");
          if (all_lanes(f0 == ep && f1 == ep && f2 == ep && f3 == ep)) break;
          __builtin_amdgcn_s_sleep(12);
        }
        lds_set(SY_AUX, 1);
        STAMP(9);
      }
      const unsigned baseAP = lds_get(SY_BAP + (lane & 3));
      lds_wait_ge(SY_K3D, 4);
      STAMP(5);
      const int rot5 = C_ROT5(b), et = C_DN(b);
      auto wdn = [&](int n0, int n1) {
#pragma unroll 1
        for (int n = n0; n < n1; ++n) {
          int kb = (n >> 1) + rot5; kb = (kb >= 14 ? kb - 14 : kb) + 14 * (n & 1);
          int sn = n + 16; sn = sn >= 38 ? sn - 38 : sn >= 19 ? sn - 19 : sn;
          const unsigned so = WS_WDT + (unsigned)(et * 28 + kb) * 8192u, sl = (unsigned)sn * 8192u;
          dma4c(sws, vl, so, sl); dma4c(sws, vl, so + 4096, sl + 4096);
          WAITV(8);
        }
      };
      lds_wait_ge(SY_WIS, 7);
      STAMP(13);
      cnt_wait(ws + WS_CAP + (4 * tp4 + lane) * 256, baseAP, lane, 4);
      STAMP(6);
      auto ablk = [&](int h0, int h1) {
#pragma unroll 1
        for (int hh = h0; hh < h1; ++hh) {
          const unsigned so = WS_AP + (unsigned)(4 * tp4 + (((hh >> 1) + ft) & 3)) * 32768u + (hh & 1) * 16384, sl = (unsigned)hh * 16384u;
#pragma unroll
          for (int q = 0; q < 4; ++q) dma4c(sws, vl, so + q * 4096, sl + q * 4096);
        }
      };
      ablk(0, 3);
      WAITV(16); lds_set(SY_ADONE + 0, 1);
      STAMP(7);
      ablk(3, 4);
      u32x2 sp[2][8];
#pragma unroll
      for (int hf = 0; hf < 2; ++hf)
#pragma unroll
        for (int e = 0; e < 8; ++e)
          sp[hf][e] = __builtin_amdgcn_raw_buffer_load_b64(rws, lane * 8 + hf * 512, WS_STATS + (unsigned)(e * 2048 + 128 * tp4) * 8u, 16);
      CFENCE();
      WAITV(16); lds_set(SY_ADONE + 1, 1);
      STAMP(16);
#pragma unroll
      for (int hf = 0; hf < 2; ++hf) {
        float mu = 0.f, m2 = 0.f;
#pragma unroll
        for (int e = 0; e < 8; ++e) { mu += u2f(sp[hf][e][0]); m2 += u2f(sp[hf][e][1]); }
        mu *= 0.125f;
        float dv = 0.f;
#pragma unroll
        for (int e = 0; e < 8; ++e) { const float d = u2f(sp[hf][e][0]) - mu; dv = fmaf(d, d, dv); }
        const float rstd = __builtin_amdgcn_rsqf(fmaf(dv, 64.f, m2) * (1.f / 512.f) + 1e-5f);
        const f32x2 o = {rstd, -mu * rstd};
        *(LAS f32x2*)(L_ST + (64 * hf + lane) * 8) = o;
      }
      WAITL0(); lds_set(SY_STAT, 1);
      STAMP(17);
      ablk(4, 7);
      WAITV(16); lds_set(SY_ADONE + 2, 1);
      STAMP(18);
      ablk(7, 8);
      WAITV(0); lds_set(SY_ADONE + 3, 1);
      STAMP(19);
      wdn(0, 3);
      lds_wait_ge(SY_UFIN + 0, 7); wdn(3, 7);
      lds_wait_ge(SY_UFIN + 1, 7); wdn(7, 11);
      lds_set(SY_WL + 0, 1);
      STAMP(20);
      lds_wait_ge(SY_UFIN + 2, 7); wdn(11, 15);
      lds_wait_ge(SY_UFIN + 3, 7); wdn(15, 19);
      WAITV(0); lds_set(SY_WL + 1, 1);
      STAMP(8);
      lds_wait_ge(SY_PROG + 0, 4);
      wdn(19, 27);
      lds_set(SY_WL + 2, 1);
      STAMP(21);
      lds_wait_ge(SY_PROG + 1, 4);
      wdn(27, 28);
      WAITV(0); lds_set(SY_WL + 3, 1);
      STAMP(22);
    }
  } else {
    const int w = wv;
    unsigned epoch, basePQ, baseG;
    {
      const int b = b0;
      const unsigned lane = lane0, q = lane >> 4, r16 = lane & 15;
      u32x4 xa, xb;
      asm volatile("global_load_dwordx4 %0, %2, off nt\n\tglobal_load_dwordx4 %1, %2, off offset:1024 nt" : "=&v"(xa), "=&v"(xb) : "v"((const char*)p.x + (8 * b + 2 * w) * 1024 + lane * 16) : "memory");
      unsigned sv = 0, ev;
      STAMP(28);
      asm volatile("global_load_dword %0, %1, off sc1" : "=&v"(ev) : "v"(ws + WS_NONCE) : "memory");
      {
        const unsigned off = lane < 2 ? (lane ? WS_CG : WS_CPQ) + (2 * C_TB64(b) + (w >> 1)) * 256 : WS_CAP + (4 * C_TP4(b) + lane - 2) * 256;
        if (lane < (w == 0 ? 6u : 2u)) asm volatile("global_atomic_add %0, %1, %2, off sc0" : "=&v"(sv) : "v"(ws + off), "v"(0u) : "memory");
      }
      if (threadIdx.x < SY_N) lds_set(threadIdx.x, 0);
      WAITL0(); BAR();
      __builtin_amdgcn_s_setprio(1);
      asm volatile("s_waitcnt vmcnt(2)" : "+v"(xa), "+v"(xb)::"memory");
      STAMP(13);
      {
        const bf2 one = __builtin_bit_cast(bf2, 0x3f803f80u);
        float sa = 0.f, sb = 0.f, qa = 0.f, qb = 0.f;
#pragma unroll
        for (int i = 0; i < 4; ++i) {
          const unsigned ua = xa[i], ub = xb[i];
          const bf2 pa = __builtin_bit_cast(bf2, ua), pb = __builtin_bit_cast(bf2, ub);
          sa = __builtin_amdgcn_fdot2_f32_bf16(pa, one, sa, false); qa = __builtin_amdgcn_fdot2_f32_bf16(pa, pa, qa, false);
          sb = __builtin_amdgcn_fdot2_f32_bf16(pb, one, sb, false); qb = __builtin_amdgcn_fdot2_f32_bf16(pb, pb, qb, false);
        }
        sa = wave_sum(sa) * (1.f / 512.f); sb = wave_sum(sb) * (1.f / 512.f);
        qa = wave_sum(qa) * (1.f / 512.f); qb = wave_sum(qb) * (1.f / 512.f);
        qa = __builtin_amdgcn_rsqf(__builtin_fmaxf(fmaf(-sa, sa, qa), 0.f) + 1e-5f); qb = __builtin_amdgcn_rsqf(__builtin_fmaxf(fmaf(-sb, sb, qb), 0.f) + 1e-5f);
        sa *= -qa; sb *= -qb;
        u32x4 na, nb;
#pragma unroll
        for (int i = 0; i < 4; ++i) {
          na[i] = pk2(fmaf(bflo(xa[i]), qa, sa), fmaf(bfhi(xa[i]), qa, sa));
          nb[i] = pk2(fmaf(bflo(xb[i]), qb, sb), fmaf(bfhi(xb[i]), qb, sb));
        }
        *(LAS u32x4*)(L_LN + (2 * w) * 1088 + lane * 16) = na;
        *(LAS u32x4*)(L_LN + (2 * w + 1) * 1088 + lane * 16) = nb;
      }
      CFENCE();
      if (lane == 0) lds_inc(SY_LN);
      lds_wait_ge(SY_LN, 4);
      {
        const unsigned la = L_LN + (r16 >> 2) * 1088 + (128 * w + 16 * q + 4 * (lane & 3)) * 2;
#pragma unroll
        for (int i = 0; i < 2; ++i) {
          const bf16x8 f = lds_tr2(la + i * 128, la + i * 128 + 4 * 1088);
          __builtin_amdgcn_raw_buffer_store_b128(__builtin_bit_cast(u32x4, f), rws, lane * 16, WS_NT + (unsigned)((2 * w + i) * 256 + b) * 1024u, 16);
        }
      }
      STAMP(14);
      asm volatile("s_waitcnt vmcnt(0)" : "+v"(sv), "+v"(ev)::"memory");
      basePQ = __builtin_amdgcn_readlane(sv, 0); baseG = __builtin_amdgcn_readlane(sv, 1);
      epoch = RFL(ev) + 1u;
      unsigned old = 0;
      if (lane == 0) old = lds_inc(SY_ND);
      if (RFL(old) == 3) round_set(ws, 0, b, lane, epoch);
      STAMP(1);
      if (w == 0) { if (lane >= 2 && lane < 6) lds_set(SY_BAP + lane - 2, sv); if (lane == 6) lds_set(SY_EP, epoch); }
      {
        const char* fp = round_word(ws, 0, b, w, lane);
_Pragma("unroll 1") for (int sp = 0; sp < SPIN_CAP; ++sp)
          if (all_lanes(atomic_peek(fp) == epoch)) break;
      }
#pragma unroll
      for (int s = 0; s < 4; ++s) dma2c(sws, lane * 16, WS_NT + (unsigned)(C_DN(b) * 32 + C_J0(b) + s) * 8192u + w * 2048, s * L_STAGE + 16384 + w * 2048);
      BAR();
      STAMP(2);
      const unsigned m0 = (unsigned)(128 * (b & 3) + 32 * w + (lane >> 3)) * 6144u + (lane & 7) * 16 + (b < 192 ? (b >> 2) * 128 : 0x40000000);
      const u32x4 sM = mk_srd(p.wmod, 512u * 3072u * 2u);
      dma2n(sM, m0, m0 + 8 * 6144 - 1024, 0, L_WM + w * 4096);
      dma2n(sM, m0 + 16 * 6144, m0 + 24 * 6144 - 1024, 0, L_WM + w * 4096 + 2048);
      dma1w(mk_srd(p.c, 1024u), lane * 4, (b & 3) * 256, L_HK + w * 256);
      const unsigned h0 = (unsigned)(64 * (b & 7) + 16 * w + r16) * 1024u + q * 16 + (b >= 128 ? 0 : 0x40000000);
      lds_wait_ge(SY_ND, 4);
      dma2n(mk_srd(b >= 192 ? p.wdim : p.wdre, 512u * 512u * 2u), h0, h0 + 64 - 1024, ((b >> 3) & 7) * 128, L_LN + w * 2048);
    }

    u32x2 xr[4];
    {
      f32x4 acc[2][4] = {};
      bf16x8 N[2][8], Wf[2][4];
      const unsigned nb0 = (lane0 >> 4) * 1024 + (lane0 & 15) * 16 + 16384;
      const unsigned wa0 = (32 * w + (lane0 & 15)) * 128 + (((lane0 >> 4) ^ ((lane0 >> 1) & 7)) * 16);
      const unsigned nbA = opv(nb0), nbB = opv(nb0 + 2 * L_STAGE), waA = opv(wa0), waB = opv(wa0 + 2 * L_STAGE), wbA = opv(wa0 ^ 64), wbB = opv((wa0 ^ 64) + 2 * L_STAGE);
      auto rd = [&](int s) {
        const unsigned o = (s & 1) * L_STAGE;
        const unsigned nb = (s & 2) ? nbB : nbA, wa = (s & 2) ? waB : waA, wb = (s & 2) ? wbB : wbA;
        Wf[s & 1][0] = lds_frag(wa + o); Wf[s & 1][1] = lds_frag(wa + o + 2048);
#pragma unroll
        for (int i = 0; i < 4; ++i) N[s & 1][i] = lds_frag(nb + o + i * 256);
        Wf[s & 1][2] = lds_frag(wb + o); Wf[s & 1][3] = lds_frag(wb + o + 2048);
#pragma unroll
        for (int i = 0; i < 4; ++i) N[s & 1][4 + i] = lds_frag(nb + o + 4096 + i * 256);
      };
      auto mm = [&](int s) {
#pragma unroll
        for (int kk = 0; kk < 2; ++kk)
#pragma unroll
          for (int i = 0; i < 4; ++i) {
            acc[0][i] = MFMA16(N[s & 1][4 * kk + i], Wf[s & 1][2 * kk], acc[0][i]);
            acc[1][i] = MFMA16(N[s & 1][4 * kk + i], Wf[s & 1][2 * kk + 1], acc[1][i]);
          }
      };
      unsigned pk1 = 0;
      WAITV(13); BAR();
      STAMP(3);
      rd(0);
      WAITL0();
      WAITV(11);
      static_for<1, 32>([&](auto sc) {
        constexpr int s = decltype(sc)::value;
        SCHED(); BAR(); SCHED();
        rd(s);
        mm(s - 1);
#pragma unroll
        for (int i = 0; i < 12; ++i) { SGB(0x8, 1); SGB(0x100, 1); }
        SGB(0x8, 4);
        SCHED();
        WAITL0();
        SCHED();
        if constexpr (s == 1) {
          WAITV(9);
        } else if constexpr (s == 2) {
          WAITV(7);
        } else if constexpr (s == 3) {
          if (ops(b0) == 0 && w == 0 && lane0 == 0) flag_st(ws + WS_NONCE, epoch);
        } else if constexpr (s == H1) {
          const int b = ops(b0);
          const unsigned lane = opv(lane0);
          WAITV(0);
          LAS unsigned* cp = (LAS unsigned*)(L_HK + w * 256 + lane * 4);
          const unsigned cc = *cp;
          const bf16x8 f0 = lds_frag(L_LN + w * 2048 + lane * 16), f1 = lds_frag(L_LN + w * 2048 + 1024 + lane * 16);
          const float c0 = bflo(cc), c1 = bfhi(cc);
          *cp = pk2(c0 * __builtin_amdgcn_rcpf(1.f + __builtin_amdgcn_exp2f(c0 * -1.44269504f)), c1 * __builtin_amdgcn_rcpf(1.f + __builtin_amdgcn_exp2f(c1 * -1.44269504f)));
          if (b >= 128) {
            const unsigned so = WS_WDH + (unsigned)(((b & 7) * 16 + ((b >> 3) & 15)) * 8 + w) * 1024u;
            __builtin_amdgcn_raw_buffer_store_b128(__builtin_bit_cast(u32x4, f0), rws, lane * 16, so, 16);
            __builtin_amdgcn_raw_buffer_store_b128(__builtin_bit_cast(u32x4, f1), rws, lane * 16, so + 4096, 16);
          }
        } else if constexpr (s == H1 + 1) {
          const int b = ops(b0);
          const unsigned lane = opv(lane0), q = lane >> 4, r16 = lane & 15;
          f32x4 d = {}, d2 = {};
          const unsigned ma = L_WM + (8 * q + (r16 >> 2)) * 128 + (16 * w + 4 * (lane & 3)) * 2;
#pragma unroll
          for (int k = 0; k < 4; k += 2) {
            d = MFMA16(lds_frag(L_HK + w * 256 + (32 * k + 8 * q) * 2), lds_tr2(ma + k * 4096, ma + k * 4096 + 512), d);
            d2 = MFMA16(lds_frag(L_HK + w * 256 + (32 * k + 32 + 8 * q) * 2), lds_tr2(ma + k * 4096 + 4096, ma + k * 4096 + 4608), d2);
          }
          if (b < 192 && lane < 16) __builtin_amdgcn_raw_buffer_store_b32(f2u(d[0] + d2[0]), rws, lane * 4, WS_MODP + (unsigned)((b & 3) * 3072 + (b >> 2) * 64 + 16 * w) * 4u, 16);
          WAITL0();
        } else if constexpr (s == H1 + 4) {
          WAITV(0);
        } else if constexpr (s == H1 + 5) {
          if (w == 0) round_set(ws, 1, ops(b0), opv(lane0), epoch);
        } else if constexpr (s == H2) {
          const int b = ops(b0);
          const unsigned lane = opv(lane0);
          if (b < 224) {
            const unsigned g0 = (unsigned)(64 * (b / 28) + 16 * w + (lane >> 3)) * 3584u + (lane & 7) * 16;
            dma2n(mk_srd(p.wg, 512u * 1792u * 2u), g0, g0 + 8 * 3584 - 1024, (b % 28) * 128, L_AUX + w * 2048);
            dma2n(mk_srd(p.wu, 512u * 1792u * 2u), g0, g0 + 8 * 3584 - 1024, (b % 28) * 128, L_AUX + 8192 + w * 2048);
            const unsigned d0 = (unsigned)(64 * (b >> 3) + 16 * w + (lane >> 3)) * 1024u + (lane & 7) * 16;
            dma2n(mk_srd(p.wdn, 1792u * 512u * 2u), d0, d0 + 8 * 1024 - 1024, (b & 7) * 128, L_AUX + 16384 + w * 2048);
          }
          asm volatile("global_atomic_add %0, %1, %2, off sc0" : "=&v"(pk1) : "v"(round_word(ws, 1, b, w, lane)), "v"(0u) : "memory");
        } else if constexpr (s == H2 + 4) {
          asm volatile("s_waitcnt vmcnt(0)" : "+v"(pk1)::"memory");
          if (!all_lanes(pk1 == epoch)) {
            const char* fp = round_word(ws, 1, ops(b0), w, opv(lane0));
_Pragma("unroll 1") for (int sp = 0; sp < SPIN_CAP; ++sp) {
              if (all_lanes(atomic_peek(fp) == epoch)) break;
              __builtin_amdgcn_s_sleep(8);
            }
          }
          CFENCE();
        } else if constexpr (s == H2 + 5 || s == H2 + 6) {
          const int b = ops(b0);
          const unsigned lane = opv(lane0), q = lane >> 4, r16 = lane & 15;
          if constexpr (s == H2 + 5) {
            const int dn = C_DN(b);
            const int cb0 = (w == 3 ? 32 : 8 * w) + dn;
#pragma unroll
            for (int k = 0; k < 4; ++k) {
              dma1c(sws, lane * 16, WS_MODP + (unsigned)(k * 3072 + 1536 + 256 * w) * 4u, L_WM + w * 4096 + k * 1024);
              dma1w(sws, lane * 4, WS_MODP + (unsigned)(k * 3072 + cb0 * 64) * 4u, L_LN + w * 2048 + k * 256);
              dma1w(sws, (lane >> 5) * 16384 + (lane & 31) * 4, WS_RSP + (unsigned)((4 * k + C_PART(b)) * 2048 + 128 * C_TP(b) + 32 * w) * 4u, L_LN + w * 2048 + 1024 + k * 256);
            }
            dma1w(sws, lane * 4, WS_MODP + (unsigned)(w * 3072 + (40 + dn) * 64) * 4u, L_HK + w * 256);
          }
          if (b < 224) {
            const unsigned ga = L_AUX + (8 * q + (r16 >> 2)) * 128 + (16 * w + 4 * (lane & 3)) * 2;
#pragma unroll
            for (int k = 2 * (s - H2 - 5); k < 2 * (s - H2 - 5) + 2; ++k) {
              const unsigned a = ga + (k >> 1) * 4096 + (k & 1) * 8192;
              __builtin_amdgcn_raw_buffer_store_b128(__builtin_bit_cast(u32x4, lds_tr2(a, a + 512)), rws, lane * 16 + k * 1024, WS_WT + (unsigned)((4 * (b % 28) + w) * 32 + 4 * (b / 28)) * 1024u, 16);
            }
            const unsigned a = ga + 16384 + (s - H2 - 5) * 4096;
            __builtin_amdgcn_raw_buffer_store_b128(__builtin_bit_cast(u32x4, lds_tr2(a, a + 512)), rws, lane * 16, WS_WDT + (unsigned)(((b & 7) * 28 + (b >> 3)) * 8 + 4 * (s - H2 - 5) + w) * 1024u, 16);
          }
        } else if constexpr (s == H2 + 8) {
          const unsigned lane = opv(lane0);
          if (ops(b0) < 224) WAITV(6); else WAITV(0);
          const unsigned va = L_WM + w * 4096 + lane * 16;
          const f32x4 v = (lds_f4(va) + lds_f4(va + 1024)) + (lds_f4(va + 2048) + lds_f4(va + 3072));
          const LAS float* mp = (const LAS float*)(L_LN + w * 2048 + lane * 4);
          float m0 = rbf((mp[0] + mp[64]) + (mp[128] + mp[192]));
          const float rs = sum32((mp[256] + mp[320]) + (mp[384] + mp[448]));
          const float one = w >= 2 ? 1.f : 0.f;
          const u32x2 vb = {pk2(rbf(v[0]) + one, rbf(v[1]) + one), pk2(rbf(v[2]) + one, rbf(v[3]) + one)};
          *(LAS u32x2*)(L_V + w * 512 + lane * 8) = vb;
          if (w & 1) m0 = rbf(1.f + m0);
          if (w < 2 && C_PART(ops(b0))) m0 = -m0;
          *(LAS float*)(L_MOD + w * 256 + lane * 4) = m0;
          if (lane < 32) *(LAS float*)(L_RS + (32 * w + lane) * 4) = rs;
          WAITL0();
        } else if constexpr (s == H2 + 9) {
          if (w == 0) {
            const LAS float* mp = (const LAS float*)(L_HK + opv(lane0) * 4);
            *(LAS float*)(L_MOD + 1024 + opv(lane0) * 4) = rbf((mp[0] + mp[64]) + (mp[128] + mp[192]));
            WAITL0();
          }
        } else if constexpr (s == H2 + 10) {
          WAITV(0);
        } else if constexpr (s == H2 + 11) {
          if (w == 0) round_set(ws, 2, ops(b0), opv(lane0), epoch);
        } else if constexpr (s == 31) {
          const int b = ops(b0);
          const unsigned lane = opv(lane0);
          const __amdgpu_buffer_rsrc_t rx = mk_rsrc(p.x, 2048u * 512u * 2u);
#pragma unroll
          for (int e = 0; e < 4; ++e)
            xr[e] = __builtin_amdgcn_raw_buffer_load_b64(rx, (lane & 15) * 1024 + (lane >> 4) * 8 + e * 32, (unsigned)(64 * C_TB64(b) + 16 * w) * 1024u + C_DN(b) * 128, 0);
        }
      });
      const unsigned lane = opv(lane0), q = lane >> 4, r16 = lane & 15;
      const unsigned tb = opv(L_MOD + q * 16);
      f32x4 sa[4], c1[4];
#pragma unroll
      for (int i = 0; i < 4; ++i) { sa[i] = lds_f4(tb + i * 64); c1[i] = lds_f4(tb + 256 + i * 64); }
      const float rs0 = *(const LAS float*)(L_RS + (32 * w + r16) * 4), rs1 = *(const LAS float*)(L_RS + (32 * w + 16 + r16) * 4);
      SCHED();
      mm(31);
      SCHED();
      STAMP(4);
      const int b = ops(b0);
      const unsigned vo = ((q & 1) * 2 + (q >> 1)) * 256 + r16 * 16;
      const unsigned so = WS_PQ + (unsigned)((8 * C_TP(b) + 2 * w) * 32 + C_PART(b) * 16 + 2 * C_DN(b)) * 1024u;
#pragma unroll
      for (int tt = 0; tt < 2; ++tt) {
        const float rs = tt ? rs1 : rs0;
        unsigned pk[8];
#pragma unroll
        for (int i = 0; i < 4; ++i) {
          pk[2 * i] = pk2(fmaf(acc[tt][i][0], c1[i][0], rs * sa[i][0]), fmaf(acc[tt][i][1], c1[i][1], rs * sa[i][1]));
          pk[2 * i + 1] = pk2(fmaf(acc[tt][i][2], c1[i][2], rs * sa[i][2]), fmaf(acc[tt][i][3], c1[i][3], rs * sa[i][3]));
        }
#pragma unroll
        for (int pr = 0; pr < 2; ++pr)
          __builtin_amdgcn_raw_buffer_store_b128(merge16(pk[4 * pr], pk[4 * pr + 1], pk[4 * pr + 2], pk[4 * pr + 3]), rws, vo + pr * 1024, so + tt * 32768, 16);
      }
      WAITV(0);
      if (lane == 0) cnt_add(ws + WS_CPQ + (4 * C_TP(b) + w) * 256);
      STAMP(5);
    }

    float x1[16];
    {
      const int b = ops(b0);
      const unsigned lane = opv(lane0), q = lane >> 4, r16 = lane & 15, vl = lane * 16;
      const int tb64 = C_TB64(b), et = C_DN(b), tb16 = 4 * tb64 + w, t32c = 2 * tb64 + (w >> 1);
      lds_wait4(SY_WD);
      STAMP(15);
      cnt_wait(ws + WS_CPQ + t32c * 256, basePQ, lane, 1);
      STAMP(6);
      const int kh = w & 1;
      bf16x8 pq[32];
#pragma unroll
      for (int n = 0; n < 32; ++n)
        pq[n] = __builtin_bit_cast(bf16x8, __builtin_amdgcn_raw_buffer_load_b128(rws, vl + ((n >> 1) & 1) * 1024, WS_PQ + (unsigned)((2 * t32c + (n & 1)) * 32 + 16 * kh + (((n >> 2) + et) & 7) * 2) * 1024u, 16));
      SCHED();
      f32x4 y2[2][4] = {};
      bf16x8 F[64];
      unsigned fa;
      auto rdw = [&](int i) {
        if ((i & 7) == 0) {
          int nl = ((((i >> 3) + et) & 7) + 8 * kh - 4 * (tb64 & 3)) & 15;
          nl = nl >= 7 ? nl - 7 : nl + 12;
          fa = opv(vl + (unsigned)nl * 8192u);
        }
        F[i] = lds_frag(fa + (i & 7) * 1024);
      };
      static_for<0, 6>([&](auto ic) { rdw(decltype(ic)::value); });
      SCHED();
      static_for<0, 64>([&](auto ic) {
        constexpr int i = decltype(ic)::value;
        y2[0][i & 3] = MFMA16(F[i], pq[2 * (i >> 2)], y2[0][i & 3]);
        SCHED();
        y2[1][i & 3] = MFMA16(F[i], pq[2 * (i >> 2) + 1], y2[1][i & 3]);
        SCHED();
        if constexpr (i + 6 < 64) rdw(i + 6);
        if constexpr (i == PQ_SIG) { if (lane == 0) lds_inc(SY_PQL); }
        SCHED();
      });
      f32x4 y[4];
      pair_reduce(y, y2, 9 * 8192, SY_X3, w, lane);
      STAMP(7);
      const unsigned tb = opv(L_MOD + 512 + q * 16);
      f32x4 ga[4], c1[4];
#pragma unroll
      for (int e = 0; e < 4; ++e) { ga[e] = lds_f4(tb + e * 64); c1[e] = lds_f4(tb + 256 + e * 64); }
      unsigned pk[8];
      float sm = 0.f;
#pragma unroll
      for (int e = 0; e < 4; ++e) {
        x1[4 * e] = fmaf(ga[e][0], y[e][0], bflo(xr[e][0])); x1[4 * e + 1] = fmaf(ga[e][1], y[e][1], bfhi(xr[e][0]));
        x1[4 * e + 2] = fmaf(ga[e][2], y[e][2], bflo(xr[e][1])); x1[4 * e + 3] = fmaf(ga[e][3], y[e][3], bfhi(xr[e][1]));
        pk[2 * e] = pk2(x1[4 * e] * c1[e][0], x1[4 * e + 1] * c1[e][1]); pk[2 * e + 1] = pk2(x1[4 * e + 2] * c1[e][2], x1[4 * e + 3] * c1[e][3]);
        sm += (x1[4 * e] + x1[4 * e + 1]) + (x1[4 * e + 2] + x1[4 * e + 3]);
      }
      const unsigned vo = ((q & 1) * 2 + (q >> 1)) * 256 + r16 * 16;
#pragma unroll
      for (int pr = 0; pr < 2; ++pr)
        __builtin_amdgcn_raw_buffer_store_b128(merge16(pk[4 * pr], pk[4 * pr + 1], pk[4 * pr + 2], pk[4 * pr + 3]), rws, vo + pr * 1024, WS_AP + (unsigned)(tb16 * 16 + 2 * et) * 1024u, 16);
      const float mu = sum32(sum16(sm)) * (1.f / 64.f);
      float m2 = 0.f;
#pragma unroll
      for (int i = 0; i < 16; ++i) { const float d = x1[i] - mu; m2 = fmaf(d, d, m2); }
      m2 = sum32(sum16(m2));
      if (lane < 16) {
        const u32x2 o = {f2u(mu), f2u(m2)};
        __builtin_amdgcn_raw_buffer_store_b64(o, rws, lane * 8, WS_STATS + (unsigned)(et * 2048 + 16 * tb16) * 8u, 16);
      }
      if (lane == 0) lds_inc(SY_K3D);
      WAITV(0);
      if (lane == 0) cnt_add(ws + WS_CAP + t32c * 256);
      STAMP(8);
    }
    __builtin_amdgcn_s_setprio(0);
    {
      const int b = ops(b0);
      const unsigned lane = opv(lane0);
      bf16x8 W[32];
      lds_wait_ge(SY_AUX, 1);
      load_w(W, rws, 7 * C_FT(b) + w, C_LOC(b) >> 2, lane);
      k4_body(W, rws, ws, w, lane, C_TP4(b), C_FT(b), C_LOC(b) >> 2);
    }
    STAMP(9);

    {
      const int b = ops(b0);
      const unsigned lane = opv(lane0), q = lane >> 4, r16 = lane & 15, vl = lane * 16;
      const int tb64 = C_TB64(b), et = C_DN(b), tb16 = 4 * tb64 + w, t32c = 2 * tb64 + (w >> 1);
      cnt_wait(ws + WS_CG + t32c * 256, baseG, lane, 1);
      STAMP(10);
      const int kh = w & 1, rot5 = C_ROT5(b);
      const unsigned gb0 = WS_G + (unsigned)(2 * t32c) * 57344u + (unsigned)kh * 28672u;
      const unsigned vy = opv(vl + 57344);
      bf16x8 g[56];
      auto ldg = [&](int m) {
        int kb = m + rot5; kb = kb >= 14 ? kb - 14 : kb;
#pragma unroll
        for (int i = 0; i < 4; ++i)
          g[4 * m + i] = __builtin_bit_cast(bf16x8, __builtin_amdgcn_raw_buffer_load_b128(rws, ((i & 1) ? vy : vl) + (i >> 1) * 1024, gb0 + (unsigned)kb * 2048u, 16));
      };
      static_for<0, G_WIN>([&](auto mc) { ldg(decltype(mc)::value); });
      f32x4 y2[2][4] = {};
      bf16x8 F[112];
      unsigned fa;
      auto rdw = [&](int i) {
        if ((i & 7) == 0) {
          const int m = i >> 3;
          if (m == 5) lds_wait_ge(SY_WL + 1, 1);
          if (m == 9) lds_wait_ge(SY_WL + 2, 1);
          if (m == 13) lds_wait_ge(SY_WL + 3, 1);
          const int s0 = (16 + 2 * m) % 19, s1 = (17 + 2 * m) % 19;
          fa = opv(vl + (unsigned)(kh ? s1 : s0) * 8192u);
        }
        F[i] = lds_frag(fa + (i & 7) * 1024);
      };
      lds_wait_ge(SY_WL + 0, 1);
      static_for<0, 6>([&](auto ic) { rdw(decltype(ic)::value); });
      SCHED();
      static_for<0, 112>([&](auto ic) {
        constexpr int i = decltype(ic)::value;
        y2[0][i & 3] = MFMA16(F[i], g[2 * (i >> 2)], y2[0][i & 3]);
        SCHED();
        y2[1][i & 3] = MFMA16(F[i], g[2 * (i >> 2) + 1], y2[1][i & 3]);
        SCHED();
        if constexpr (i + 6 < 112) rdw(i + 6);
        if constexpr ((i & 7) == 7 && (i >> 3) + G_WIN < 14) ldg((i >> 3) + G_WIN);
        if constexpr (i == 31 || i == 47) { if (lane == 0) lds_inc(SY_PROG + (i == 47)); }
        if constexpr (i == 31 || i == 55 || i == 87) STAMP(24 + (i >> 5) + (i == 55));
        SCHED();
      });
      lds_wait_ge(SY_PROG + 1, 4);
      f32x4 y[4];
      pair_reduce(y, y2, 6 * 8192, SY_X5, w, lane);
      STAMP(11);
      const unsigned tb = opv(L_MOD + 1024 + q * 16);
      unsigned pk[8];
#pragma unroll
      for (int e = 0; e < 4; ++e) {
        const f32x4 gb = lds_f4(tb + e * 64);
        pk[2 * e] = pk2(fmaf(gb[0], y[e][0], x1[4 * e]), fmaf(gb[1], y[e][1], x1[4 * e + 1]));
        pk[2 * e + 1] = pk2(fmaf(gb[2], y[e][2], x1[4 * e + 2]), fmaf(gb[3], y[e][3], x1[4 * e + 3]));
      }
      const __amdgpu_buffer_rsrc_t ro = mk_rsrc(p.out, 2048u * 512u * 2u);
#pragma unroll
      for (int pr = 0; pr < 2; ++pr)
        __builtin_amdgcn_raw_buffer_store_b128(merge16(pk[4 * pr], pk[4 * pr + 1], pk[4 * pr + 2], pk[4 * pr + 3]), ro, r16 * 1024 + (q & 1) * 32 + (q >> 1) * 16 + pr * 64,
                                               (unsigned)(16 * tb16) * 1024u + et * 128, 16);
    }
    STAMP(12);
  }
}

#if defined(__HIP_DEVICE_COMPILE__)
#pragma clang attribute pop
#endif

extern "C" void kernel_launch(void* const* d_in, const int* in_sizes, int n_in, void* d_out, int out_size, void* d_ws, size_t ws_size,
                              hipStream_t stream) {
  Params p;
  p.x = (const u16*)d_in[0]; p.wsre = (const u16*)d_in[1]; p.wsim = (const u16*)d_in[2];
  p.wdre = (const u16*)d_in[3]; p.wdim = (const u16*)d_in[4];
  p.wg = (const u16*)d_in[6]; p.wu = (const u16*)d_in[7]; p.wdn = (const u16*)d_in[8];
  p.c = (const u16*)d_in[9]; p.wmod = (const u16*)d_in[10];
  p.out = (u16*)d_out; p.ws = (char*)d_ws;
  (void)hipFuncSetAttribute((const void*)fused_block, hipFuncAttributeMaxDynamicSharedMemorySize, LDS_BYTES);
  fused_block<<<256, 512, LDS_BYTES, stream>>>(p);
}
```

```cpp
#include <hip/hip_runtime.h>

typedef unsigned short u16;
typedef unsigned long long u64;
typedef short bf16x8 __attribute__((ext_vector_type(8)));
typedef short s16x4 __attribute__((ext_vector_type(4)));
typedef __bf16 bf2 __attribute__((ext_vector_type(2)));
typedef float f32x16 __attribute__((ext_vector_type(16)));
typedef float f32x4 __attribute__((ext_vector_type(4)));
typedef float f32x2 __attribute__((ext_vector_type(2)));
typedef unsigned u32x4 __attribute__((ext_vector_type(4)));
typedef unsigned u32x2 __attribute__((ext_vector_type(2)));
#define DI __device__ __forceinline__
#define LAS __attribute__((address_space(3)))
#define GAS __attribute__((address_space(1)))

constexpr unsigned WS_NONCE = 0;
constexpr unsigned WS_FLAG = 131072;
constexpr unsigned WS_RND = 147456;
constexpr unsigned WS_CPQ = 16384;
constexpr unsigned WS_CAP = 32768;
constexpr unsigned WS_CG = 49152;
constexpr unsigned WS_MODP = 65536;
constexpr unsigned WS_STATS = 393216;
constexpr unsigned WS_NT = 1u << 20;
constexpr unsigned WS_PQ = 4u << 20;
constexpr unsigned WS_AP = 8u << 20;
constexpr unsigned WS_G = 10u << 20;
constexpr unsigned WS_WT = 18u << 20;
constexpr unsigned WS_WDT = 22u << 20;
constexpr unsigned WS_WDH = 24u << 20;
constexpr unsigned WS_DIAG = 25u << 20;
constexpr unsigned WS_BYTES = 26u << 20;

constexpr unsigned L_STAGE = 24576;
constexpr unsigned L_AUX = 98304;
constexpr unsigned L_LN = 16384;
constexpr unsigned L_WH = 122880;
constexpr unsigned L_WM = 131072;
constexpr unsigned L_X = 147456;
constexpr unsigned L_TAB = 155648;
constexpr unsigned L_MOD = L_TAB;
constexpr unsigned L_V = L_TAB + 1280;
constexpr unsigned L_ST = L_TAB + 3328;
constexpr unsigned L_SY = L_TAB + 4352;
constexpr unsigned LDS_BYTES = 163840;
enum { SY_ADONE = 0, SY_STAT = 4, SY_UFIN = 5, SY_GSIG = 9, SY_WL = 10, SY_AREQ = 13, SY_PROG = 14, SY_LN = 16, SY_ND = 17, SY_PQGO = 18, SY_PQL = 19, SY_WD = 20, SY_X3 = 24, SY_X5 = 28, SY_AGO = 32, SY_K3D = 33, SY_SEEN = 34, SY_SIDE = 35, SY_V = 36, SY_PROG2 = 37, SY_WDT = 40, SY_R31 = 41, SY_N = 42, SY_BAP = 44 };
constexpr unsigned L_HK = L_TAB + 6912;
#define PRE_S 2
#define L_PACE 10
#define G_WIN 8
#define A_DELAY 8
#define HC 12
#define LATE_W 1
#define P_WIN 8
#define AROT 0
#define A_HOLD 1
#define Y_DONE SY_AREQ
#define WN_PACE 16
#define WDROT 1
#define FINE 0
#define XPRO 0
#define HALFC 0
#define RAWWD 1
#define PQ_SIG 56
#define A_PACE 8
#define W_PACE 8
#define WD_PACE 6

static_assert(HC + 7 < 28, "side job order");
constexpr int SPIN_CAP = 1 << 20;
constexpr int CODE_LINES_PER_WG = 11;

struct Params {
  const u16 *x, *wsre, *wsim, *wdre, *wdim, *wg, *wu, *wdn, *c, *wmod;
  u16* out;
  char* ws;
};

#if defined(__HIP_DEVICE_COMPILE__)
#pragma clang attribute push(__attribute__((target("no-packed-fp32-ops"))), apply_to = function)
#endif
DI float u2f(unsigned u) { return __builtin_bit_cast(float, u); }
DI unsigned f2u(float f) { return __builtin_bit_cast(unsigned, f); }
DI bool all_lanes(bool c) { return __builtin_amdgcn_ballot_w64(c) == ~0ull; }
DI u32x4 mk_srd(const void* p, unsigned bytes) {
  u64 a = (u64)p;
  u32x4 r = {(unsigned)a, (unsigned)(a >> 32) & 0xffffu, bytes, 0x00020000u};
  return r;
}
DI __amdgpu_buffer_rsrc_t mk_rsrc(const void* p, unsigned bytes) {
  return __builtin_amdgcn_make_buffer_rsrc((void*)p, 0, bytes, 0x00020000);
}
#define RFL(x) __builtin_amdgcn_readfirstlane(x)
#define BAR() __builtin_amdgcn_s_barrier()
#define WAITV(n) asm volatile("s_waitcnt vmcnt(%0)" ::"i"(n) : "memory")
#define WAITL0() __builtin_amdgcn_s_waitcnt(0xC07F)
#define CFENCE() asm volatile("" ::: "memory")

DI void dma4v(u32x4 srd, unsigned v0, unsigned v1, unsigned v2, unsigned v3, unsigned soff, unsigned m0v) {
  asm volatile("s_mov_b32 m0, %0\n\ts_nop 0\n\t"
               "buffer_load_dwordx4 %1, %5, %6 offen lds\n\t"
               "buffer_load_dwordx4 %2, %5, %6 offen offset:1024 lds\n\t"
               "buffer_load_dwordx4 %3, %5, %6 offen offset:2048 lds\n\t"
               "buffer_load_dwordx4 %4, %5, %6 offen offset:3072 lds"
               ::"s"(m0v), "v"(v0), "v"(v1), "v"(v2), "v"(v3), "s"(srd), "s"(soff) : "memory");
}
DI void dma2v(u32x4 srd, unsigned v0, unsigned v1, unsigned soff, unsigned m0v) {
  asm volatile("s_mov_b32 m0, %0\n\ts_nop 0\n\t"
               "buffer_load_dwordx4 %1, %3, %4 offen lds\n\t"
               "buffer_load_dwordx4 %2, %3, %4 offen offset:1024 lds"
               ::"s"(m0v), "v"(v0), "v"(v1), "s"(srd), "s"(soff) : "memory");
}
DI void dma2n(u32x4 srd, unsigned v0, unsigned v1, unsigned soff, unsigned m0v) {
  asm volatile("s_mov_b32 m0, %0\n\ts_nop 0\n\t"
               "buffer_load_dwordx4 %1, %3, %4 offen nt lds\n\t"
               "buffer_load_dwordx4 %2, %3, %4 offen offset:1024 nt lds"
               ::"s"(m0v), "v"(v0), "v"(v1), "s"(srd), "s"(soff) : "memory");
}
DI void dma2c(u32x4 srd, unsigned v, unsigned soff, unsigned m0v) {
  asm volatile("s_mov_b32 m0, %0\n\ts_nop 0\n\t"
               "buffer_load_dwordx4 %1, %2, %3 offen sc1 lds\n\t"
               "buffer_load_dwordx4 %1, %2, %3 offen offset:1024 sc1 lds"
               ::"s"(m0v), "v"(v), "s"(srd), "s"(soff) : "memory");
}
DI void dma4c(u32x4 srd, unsigned v, unsigned soff, unsigned m0v) {
  asm volatile("s_mov_b32 m0, %0\n\ts_nop 0\n\t"
               "buffer_load_dwordx4 %1, %2, %3 offen sc1 lds\n\t"
               "buffer_load_dwordx4 %1, %2, %3 offen offset:1024 sc1 lds\n\t"
               "buffer_load_dwordx4 %1, %2, %3 offen offset:2048 sc1 lds\n\t"
               "buffer_load_dwordx4 %1, %2, %3 offen offset:3072 sc1 lds"
               ::"s"(m0v), "v"(v), "s"(srd), "s"(soff) : "memory");
}
DI void dma1w(u32x4 srd, unsigned v, unsigned soff, unsigned m0v) {
  asm volatile("s_mov_b32 m0, %0\n\ts_nop 0\n\tbuffer_load_dword %1, %2, %3 offen sc1 lds" ::"s"(m0v), "v"(v), "s"(srd), "s"(soff) : "memory");
}
DI void dma1c(u32x4 srd, unsigned v, unsigned soff, unsigned m0v) {
  asm volatile("s_mov_b32 m0, %0\n\ts_nop 0\n\tbuffer_load_dwordx4 %1, %2, %3 offen sc1 lds" ::"s"(m0v), "v"(v), "s"(srd), "s"(soff) : "memory");
}
template <int AUX> DI void st128_(u32x4 v, __amdgpu_buffer_rsrc_t r, unsigned off) { __builtin_amdgcn_raw_buffer_store_b128(v, r, off, 0, AUX); }
#define st128(v, r, vo, so, aux) st128_<aux>((v), (r), (vo) + (so))
DI unsigned opv(unsigned v) { asm volatile("" : "+v"(v)); return v; }
DI int ops(int v) { asm volatile("" : "+s"(v)); return v; }
#define SCHED() __builtin_amdgcn_sched_barrier(0)
#define SGB(m, n) __builtin_amdgcn_sched_group_barrier((m), (n), 0)
DI bf16x8 lds_frag(unsigned a) { return *(const LAS bf16x8*)a; }
DI f32x4 lds_f4(unsigned a) { return *(const LAS f32x4*)a; }
DI bf16x8 lds_tr2(unsigned a0, unsigned a1) {
  s16x4 lo = __builtin_amdgcn_ds_read_tr16_b64_v4i16((LAS s16x4*)a0);
  s16x4 hi = __builtin_amdgcn_ds_read_tr16_b64_v4i16((LAS s16x4*)a1);
  return __builtin_shufflevector(lo, hi, 0, 1, 2, 3, 4, 5, 6, 7);
}
DI unsigned pk2(float a, float b) { bf2 p = {(__bf16)a, (__bf16)b}; return __builtin_bit_cast(unsigned, p); }
DI float bflo(unsigned u) { return u2f(u << 16); }
DI float bfhi(unsigned u) { return u2f(u & 0xffff0000u); }
DI float rbf(float a) { return (float)(__bf16)a; }
DI float sum32(float v) { auto r = __builtin_amdgcn_permlane32_swap(f2u(v), f2u(v), false, false); return u2f(r[0]) + u2f(r[1]); }
DI float sum16(float v) { auto r = __builtin_amdgcn_permlane16_swap(f2u(v), f2u(v), false, false); return u2f(r[0]) + u2f(r[1]); }
template <int CTRL> DI float dpp_add(float v) { return v + u2f(__builtin_amdgcn_update_dpp(0u, f2u(v), CTRL, 0xf, 0xf, true)); }
DI float wave_sum(float v) {
  v = dpp_add<0xB1>(v);
  v = dpp_add<0x4E>(v);
  v = dpp_add<0x141>(v);
  v = dpp_add<0x140>(v);
  return sum32(sum16(v));
}
#define MFMA16(a, b, c) __builtin_amdgcn_mfma_f32_16x16x32_bf16((a), (b), (c), 0, 0, 0)

DI unsigned atomic_peek(const char* p) {
  unsigned r, z = 0;
  asm volatile("global_atomic_add %0, %1, %2, off sc0\n\ts_waitcnt vmcnt(0)" : "=&v"(r) : "v"(p), "v"(z) : "memory");
  return r;
}
DI void cnt_add(char* p) { asm volatile("global_atomic_add %0, %1, off" ::"v"(p), "v"(1u) : "memory"); }
DI unsigned flag_ld(const char* p) { return __hip_atomic_load((GAS unsigned*)p, __ATOMIC_RELAXED, __HIP_MEMORY_SCOPE_AGENT); }
DI void flag_st(char* p, unsigned v) { asm volatile("global_atomic_swap %0, %1, off" ::"v"(p), "v"(v) : "memory"); }
DI void first_set(char* ws, int b, unsigned lane, unsigned epoch) {
  if (lane < 16) flag_st(ws + WS_FLAG + lane * 1024 + (((b >> 1) & 3) * 64 + (b >> 3) * 2 + (b & 1)) * 4, epoch);
}
DI void round_add(char* ws, int round, int b, unsigned lane) { if (lane < 16) cnt_add(ws + WS_RND + round * 8192 + lane * 512 + (b & 3) * 128); }
DI const char* round_word(const char* ws, int round, int b, unsigned shard) { return ws + WS_RND + round * 8192 + ((b >> 3) & 15) * 512 + shard * 128; }
template <unsigned N = 16, int SLEEP = 0> DI void cnt_wait(const char* p, unsigned base, int lane, int nl) {
_Pragma("unroll 1") for (int sp = 0; sp < SPIN_CAP; ++sp) {
    unsigned v = base + N;
    if (lane < nl) v = atomic_peek(p);
    if (all_lanes(v - base >= N)) break;
    if (SLEEP) __builtin_amdgcn_s_sleep(SLEEP);
  }
  CFENCE();
}
template <int SLEEP = 1> DI void lds_wait_ge(unsigned word, unsigned want) {
  volatile LAS unsigned* p = (volatile LAS unsigned*)(L_SY + word * 4);
_Pragma("unroll 1") for (int sp = 0; sp < SPIN_CAP; ++sp) {
    if (RFL(*p) >= want) break;
    if (SLEEP) __builtin_amdgcn_s_sleep(SLEEP);
  }
  CFENCE();
}
DI void lds_wait4(unsigned word) {
  volatile LAS u32x4* p = (volatile LAS u32x4*)(L_SY + word * 4);
_Pragma("unroll 1") for (int sp = 0; sp < SPIN_CAP; ++sp) {
    const u32x4 v = *p;
    if (RFL(v[0] & v[1] & v[2] & v[3])) break;
    __builtin_amdgcn_s_sleep(1);
  }
  CFENCE();
}
DI unsigned lds_get(unsigned word) { return *(volatile LAS unsigned*)(L_SY + word * 4); }
DI void lds_set(unsigned word, unsigned v) { *(volatile LAS unsigned*)(L_SY + word * 4) = v; }
DI unsigned lds_inc(unsigned word) { return __hip_atomic_fetch_add((LAS unsigned*)(L_SY + word * 4), 1u, __ATOMIC_RELAXED, __HIP_MEMORY_SCOPE_WORKGROUP); }

#define STAMP(i)
#define STAMPB(i)

template <int I> struct IC { static constexpr int value = I; };
template <int A, int B, class F> DI void static_for(F&& f) { if constexpr (A < B) { f(IC<A>{}); static_for<A + 1, B>(f); } }

DI u32x4 merge16(unsigned a0, unsigned a1, unsigned b0, unsigned b1) {
  auto s0 = __builtin_amdgcn_permlane16_swap(a0, b0, false, false);
  auto s1 = __builtin_amdgcn_permlane16_swap(a1, b1, false, false);
  const u32x4 o = {s0[0], s1[0], s0[1], s1[1]};
  return o;
}

DI void pair_reduce(f32x4 (&y)[4], const f32x4 (&y2)[2][4], unsigned scratch, unsigned word, int w, unsigned lane) {
  const bool odd = w & 1;
  const unsigned a = scratch + lane * 16;
#pragma unroll
  for (int e = 0; e < 4; ++e) *(LAS f32x4*)(a + w * 4096 + e * 1024) = odd ? y2[0][e] : y2[1][e];
  CFENCE();
  lds_set(word + w, 1);
  lds_wait_ge<0>(word + (w ^ 1), 1);
#pragma unroll
  for (int e = 0; e < 4; ++e) y[e] = (odd ? y2[1][e] : y2[0][e]) + lds_f4(a + (w ^ 1) * 4096 + e * 1024);
}

template <unsigned GO, unsigned DONE, unsigned N>
DI void load_w(bf16x8 (&W)[32], __amdgpu_buffer_rsrc_t rws, int c, int rot, unsigned lane) {
  const unsigned vl = opv(lane * 16);
#pragma unroll
  for (int k = 0; k < 32; ++k) {
    if ((k & 3) == 0) {
      if (k >= W_PACE) WAITV(W_PACE - 4);
      if (RFL(lds_get(GO))) lds_wait_ge(DONE, N);
    }
    W[k] = __builtin_bit_cast(bf16x8, __builtin_amdgcn_raw_buffer_load_b128(rws, vl + (k & 3) * 1024, WS_WT + (unsigned)(c * 32 + (((k >> 2) + rot) & 7) * 4) * 1024u, 16));
  }
  SCHED();
}
template <int N> DI float row_bcast(float v) { return u2f(__builtin_amdgcn_update_dpp(0u, f2u(v), 0x150 + N, 0xf, 0xf, true)); }

DI float geglu(float g, float u, float rstd, float nmr, float ug, float vg, float uu, float vu) {
  const float a = fmaf(g, rstd, fmaf(nmr, ug, vg));
  const float b = fmaf(u, rstd, fmaf(nmr, uu, vu));
  const float e = a * fmaf(a * a, -0.10294324f, -2.30220820f);
  return a * b * __builtin_amdgcn_rcpf(1.f + __builtin_amdgcn_exp2f(e));
}

template <bool LOADER>
DI void k4_body(const bf16x8 (&W)[32], __amdgpu_buffer_rsrc_t rws, u32x4 sws, char* ws, int wv, unsigned lane, int tp4, int ft, int rot) {
  const unsigned q = lane >> 4, r16 = lane & 15;
  const int c = 7 * ft + wv;
  f32x4 ug, vg, uu, vu;
  lds_wait_ge(SY_V, 1);
  {
    f32x4 dg = {}, du = {}, eg = {}, eu = {};
    const unsigned va = L_V + (lane & 1) * 1024 + q * 16;
    bf16x8 f[16];
#pragma unroll
    for (int k = 0; k < 16; ++k) f[k] = lds_frag(va + (unsigned)((((k >> 1) + rot) & 7) * 128 + (k & 1) * 64));
    SCHED();
#pragma unroll
    for (int k = 0; k < 16; k += 2) {
      dg = MFMA16(W[2 * k], f[k], dg); du = MFMA16(W[2 * k + 1], f[k], du);
      eg = MFMA16(W[2 * k + 2], f[k + 1], eg); eu = MFMA16(W[2 * k + 3], f[k + 1], eu);
      SCHED();
    }
    dg += eg; du += eu;
#pragma unroll
    for (int j = 0; j < 4; ++j) { vg[j] = row_bcast<0>(dg[j]); ug[j] = row_bcast<1>(dg[j]); vu[j] = row_bcast<0>(du[j]); uu[j] = row_bcast<1>(du[j]); }
  }
  SCHED();
  if (LOADER) {
    lds_wait_ge<0>(SY_AGO, 1);
    const unsigned so = WS_AP + (unsigned)(4 * tp4 + ((AROT * ft) & 3)) * 32768u + (wv - 4) * 8192;
    dma4c(sws, lane * 16, so, (wv - 4) * 8192); dma4c(sws, lane * 16, so + 4096, (wv - 4) * 8192 + 4096);
    if (lane == 0) lds_inc(SY_AREQ);
    STAMP(12);
    WAITV(0);
    if (lane == 0) lds_inc(SY_ADONE + 0);
    STAMP(13);
  }
  const unsigned gvoff = (q & 1) * 57344 + (c >> 1) * 1024 + (c & 1) * 512 + (q >> 1) * 256 + r16 * 16;
  const unsigned sb = opv(L_ST + r16 * 8);
  unsigned ab[8];
#pragma unroll
  for (int g = 0; g < 8; ++g) ab[g] = lane * 16 + (unsigned)(((g + rot) & 7) * 2048);
  lds_wait_ge<0>(SY_ADONE + 0, 4);
#pragma unroll 1
  for (int u = 0; u < 4; ++u) {
    const int tbl = (u + AROT * ft) & 3;
    STAMP(16 + 2 * u);
    f32x4 gx = {}, ux = {}, gy = {}, uy = {};
    bf16x8 A[32];
    auto rda = [&](int i) { A[i] = lds_frag(ab[i >> 2] + ((i >> 1) & 1) * 1024 + (i & 1) * 16384); };
    static_for<0, 6>([&](auto ic) { rda(decltype(ic)::value); });
    unsigned nxt, sto;
    SCHED();
    static_for<0, 32>([&](auto ic) {
      constexpr int i = decltype(ic)::value;
      if constexpr (i & 1) gy = MFMA16(W[i - 1], A[i], gy); else gx = MFMA16(W[i], A[i], gx);
      SCHED();
      if constexpr (i + 6 < 32) rda(i + 6);
      SCHED();
      if constexpr (i & 1) uy = MFMA16(W[i], A[i], uy); else ux = MFMA16(W[i + 1], A[i], ux);
      SCHED();
      if constexpr (i == 26) nxt = lds_get(SY_ADONE + u + 1);
      if constexpr (i == 27) sto = lds_get(SY_STAT);
      SCHED();
    });
    STAMP(17 + 2 * u);
    if (lane == 0) lds_inc(SY_UFIN + u);
    if (!RFL(sto)) lds_wait_ge(SY_STAT, 1);
    const f32x2 sx = *(const LAS f32x2*)(sb + tbl * 256), sy = *(const LAS f32x2*)(sb + tbl * 256 + 128);
#pragma unroll
    for (int g = 0; g < 8; ++g) ab[g] += 32768;
    float ox[4], oy[4];
#pragma unroll
    for (int j = 0; j < 4; ++j) {
      ox[j] = geglu(gx[j], ux[j], sx[0], sx[1], ug[j], vg[j], uu[j], vu[j]);
      oy[j] = geglu(gy[j], uy[j], sy[0], sy[1], ug[j], vg[j], uu[j], vu[j]);
    }
    st128(merge16(pk2(ox[0], ox[1]), pk2(ox[2], ox[3]), pk2(oy[0], oy[1]), pk2(oy[2], oy[3])), rws, gvoff,
                                           WS_G + (unsigned)(2 * (4 * tp4 + tbl)) * 57344u, 16);
    if (!RFL(nxt)) lds_wait_ge(SY_ADONE + u + 1, 1);
  }
  WAITV(0);
  unsigned old = 0;
  if (lane == 0) old = lds_inc(SY_GSIG);
  if (RFL(old) == 6 && lane < 4) cnt_add(ws + WS_CG + (4 * tp4 + lane) * 256 + HALFC * (ft >> 3) * 128);
}

#define C_XCD(b) ((b) & 7)
#define C_LOC(b) ((b) >> 3)
#define C_TP(b) (2 * C_XCD(b) + (C_LOC(b) >> 4))
#define C_PART(b) ((C_LOC(b) >> 3) & 1)
#define C_DN(b) (C_LOC(b) & 7)
#define ROTM 4
#define C_J0(b) (ROTM < 0 ? 4 * C_XCD(b) : 4 * ((C_DN(b) + C_XCD(b) + ROTM * (C_LOC(b) >> 3)) & 7))
#define C_TB64(b) (4 * C_XCD(b) + (C_LOC(b) >> 3))
#define C_TP4(b) (8 * (C_XCD(b) & 1) + (C_LOC(b) >> 2))
#define C_FT(b) (4 * (C_XCD(b) >> 1) + (C_LOC(b) & 3))
#define C_ROT4(b) (C_LOC(b) >> 2)

__global__ __launch_bounds__(512, 2) void fused_block(Params p) {
  const int b0 = blockIdx.x;
  const unsigned lane0 = threadIdx.x & 63;
  const int wv = RFL(threadIdx.x >> 6);
  char* const ws = p.ws;
  u64 pc0;
  asm volatile("s_getpc_b64 %0" : "=s"(pc0));
  STAMP(0);
  const u32x4 sws = mk_srd(ws, WS_BYTES);
  const __amdgpu_buffer_rsrc_t rws = mk_rsrc(ws, WS_BYTES);

  if (wv >= 4) {
    const int wl = wv - 4;
    const unsigned lane = lane0;
    const unsigned sw = (lane & 7) ^ (lane >> 4);
    const unsigned vl = lane * 16;
    {
      const int b = b0, j0 = C_J0(b), dn = C_DN(b);
      unsigned junk = 0;
      if (wv == 4 && lane < CODE_LINES_PER_WG)
        asm volatile("global_load_dword %0, %1, off" : "=&v"(junk) : "v"((const char*)(pc0 & ~255ull) + (C_LOC(b) * CODE_LINES_PER_WG + lane) * 128) : "memory");
      const u32x4 sA = mk_srd(C_PART(b) ? p.wsim : p.wsre, 2048u * 2048u * 2u);
      const unsigned ra = (unsigned)(128 * C_TP(b) + 32 * wl + (lane >> 3)) * 4096u;
      const unsigned va0 = ra + sw * 16, va1 = ra + 8 * 4096 + (sw ^ 4) * 16 - 1024, va2 = ra + 16 * 4096 + sw * 16 - 2048, va3 = ra + 24 * 4096 + (sw ^ 4) * 16 - 3072;
      {
        const unsigned l8 = lane >> 3, c16 = (lane & 7) * 16;
        const unsigned g0 = (unsigned)(64 * (b / 28) + 16 * wl + l8) * 3584u + c16 + (b < 224 ? 0 : 0x40000000);
        const unsigned h0 = (unsigned)(64 * (b & 7) + 16 * wl + (lane & 15)) * 1024u + (lane >> 4) * 16 + (b >= 128 ? 0 : 0x40000000);
        const unsigned m0 = (unsigned)(128 * (b & 3) + 32 * wl + l8) * 6144u + c16 + (b < 192 ? (b >> 2) * 128 : 0x40000000);
        const u32x4 sM = mk_srd(p.wmod, 512u * 3072u * 2u);
        static_for<0, 5>([&](auto ic) {
          constexpr int i = decltype(ic)::value;
          if (i == 1) BAR();
          if (i == PRE_S) lds_wait_ge<0>(SY_ND, 4);
          if (i == 0) dma2n(mk_srd(p.wg, 512u * 1792u * 2u), g0, g0 + 8 * 3584 - 1024, (b % 28) * 128, L_AUX + wl * 2048);
          if (i == 1) dma2n(mk_srd(p.wu, 512u * 1792u * 2u), g0, g0 + 8 * 3584 - 1024, (b % 28) * 128, L_AUX + 8192 + wl * 2048);
          if (i == 2 && !RAWWD) dma2n(mk_srd(b >= 192 ? p.wdim : p.wdre, 512u * 512u * 2u), h0, h0 + 64 - 1024, ((b >> 3) & 7) * 128, L_WH + wl * 2048);
          if (i == 3) dma2n(sM, m0, m0 + 8 * 6144 - 1024, 0, L_WM + wl * 4096);
          if (i == 4) dma2n(sM, m0 + 16 * 6144, m0 + 24 * 6144 - 1024, 0, L_WM + wl * 4096 + 2048);
        });
        if (PRE_S >= 5) lds_wait_ge<0>(SY_ND, 4);
#pragma unroll
        for (int s = 0; s < 4; ++s) {
          WAITV(A_PACE);
          dma4v(sA, va0, va1, va2, va3, (unsigned)(j0 + s) * 128u, s * L_STAGE + wl * 4096);
          if (s == 1) { asm volatile("s_waitcnt vmcnt(8)" : "+v"(junk)::"memory"); if (lane == 0) lds_inc(SY_SIDE); }
        }
      }
      if (XPRO) {
        const unsigned x0 = (unsigned)(64 * C_TB64(b) + 16 * wl + (lane >> 3)) * 1024u + ((lane & 7) ^ (lane >> 3)) * 16;
        dma2v(mk_srd(p.x, 2048u * 512u * 2u), x0, x0 + 8 * 1024 - 1024, dn * 128, L_X + wl * 2048);
      }
      STAMP(1);
      lds_wait_ge(SY_SIDE, 4);
      {
        const unsigned q = lane >> 4, r16 = lane & 15;
        const unsigned ga = (8 * q + (r16 >> 2)) * 128 + (16 * wl + 4 * (lane & 3)) * 2;
        bf16x8 tw[6];
#pragma unroll
        for (int k = 0; k < 4; ++k) {
          const unsigned a = L_AUX + ga + (k >> 1) * 4096 + (k & 1) * 8192;
          tw[k] = lds_tr2(a, a + 512);
        }
        tw[4] = lds_frag(L_WH + wl * 2048 + lane * 16); tw[5] = lds_frag(L_WH + wl * 2048 + 1024 + lane * 16);
        f32x4 d = {}, d2 = {};
#pragma unroll
        for (int k = 0; k < 4; k += 2) {
          d = MFMA16(lds_frag(L_HK + wl * 256 + (32 * k + 8 * q) * 2), lds_tr2(L_WM + ga + k * 4096, L_WM + ga + k * 4096 + 512), d);
          d2 = MFMA16(lds_frag(L_HK + wl * 256 + (32 * k + 32 + 8 * q) * 2), lds_tr2(L_WM + ga + k * 4096 + 4096, L_WM + ga + k * 4096 + 4608), d2);
        }
        float md = d[0] + d2[0];
        const unsigned t0 = vl + WS_WT + (unsigned)((4 * (b % 28) + wl) * 32 + 4 * (b / 28)) * 1024u + (b < 224 ? 0 : 0x40000000);
        const unsigned t1 = vl + WS_WDH + (unsigned)(((b & 7) * 16 + ((b >> 3) & 15)) * 8 + wl) * 1024u + (b >= 128 ? 0 : 0x40000000);
        const unsigned t2 = lane * 4 + ((b < 192 && lane < 16) ? 0 : 0x40000000);
        asm volatile("" : "+v"(tw[0]), "+v"(tw[1]), "+v"(tw[2]), "+v"(tw[3]), "+v"(tw[4]), "+v"(tw[5]), "+v"(md));
        lds_wait_ge<0>(SY_SEEN, 4);
        STAMP(13);
#pragma unroll
        for (int k = 0; k < 4; ++k) st128(__builtin_bit_cast(u32x4, tw[k]), rws, t0, k * 1024, 16);
        if (!RAWWD) {
          st128(__builtin_bit_cast(u32x4, tw[4]), rws, t1, 0, 16);
          st128(__builtin_bit_cast(u32x4, tw[5]), rws, t1, 4096, 16);
        }
        __builtin_amdgcn_raw_buffer_store_b32(f2u(md), rws, t2, WS_MODP + (unsigned)((b & 3) * 3072 + (b >> 2) * 64 + 16 * wl) * 4u, 16);
      }
      WAITV(RAWWD ? 5 : 7);
      BAR();
      STAMP(2);
      BAR();
      STAMPB(1);
      auto stage1 = [&](int s) {
        const unsigned j = (unsigned)(j0 + s) & 31u, sl = (unsigned)(s & 3) * L_STAGE;
        WAITV(L_PACE);
        dma2c(sws, vl, WS_NT + ((unsigned)dn * 32u + j) * 8192u + wl * 2048, sl + 16384 + wl * 2048);
      };
      auto stage2 = [&](int s) {
        const unsigned j = (unsigned)(j0 + s) & 31u, sl = (unsigned)(s & 3) * L_STAGE;
        WAITV(L_PACE);
        dma2v(sA, va0, va1, j * 128u, sl + wl * 4096);
        WAITV(L_PACE);
        dma2v(sA, va2 + 2048, va3 + 2048, j * 128u, sl + wl * 4096 + 2048);
      };
      auto stage = [&](int s) { stage1(s); stage2(s); };
      stage(4);
      BAR();
      STAMPB(2);
      stage(5);
      BAR();
      STAMPB(3);
      stage(6);
      WAITV(12); BAR();
      STAMPB(4);
#pragma unroll 1
      for (int s = 5; s <= 29; ++s) {
        stage(s + 2);
        if (s == HC + 7) {
          const LAS float* mp = (const LAS float*)(L_AUX + wl * 2048 + lane * 4);
          float m0 = rbf((mp[0] + mp[64]) + (mp[128] + mp[192]));
          const float m1 = rbf((mp[256] + mp[320]) + (mp[384] + mp[448]));
          if (wl & 1) m0 = rbf(1.f + m0);
          if (wl < 2 && C_PART(b)) m0 = -m0;
          *(LAS float*)(L_MOD + wl * 256 + lane * 4) = m0;
          if (wl == 0) *(LAS float*)(L_MOD + 1024 + lane * 4) = m1;
          WAITL0();
        }
        WAITV(12); BAR(); STAMPB(s); if ((s & 3) == 0) STAMP(23 + (s >> 2)); }
    }
    const int b = ops(b0);
    {
      const unsigned rd = (unsigned)(64 * C_DN(b) + 16 * wl + (lane >> 3)) * 1024u;
      const unsigned vd0 = rd + sw * 16, vd1 = rd + 8 * 1024 + (sw ^ 4) * 16 - 1024;
      auto wdblk = [&](int n) {
        const unsigned blk = (unsigned)(n + WDROT * 4 * (C_TB64(b) & 3)) & 15u;
        const unsigned sl = (unsigned)(n < 6 ? 12 + n : n < 15 ? n - 6 : 11) * 8192u + wl * 2048;
        if (RAWWD) dma2v(mk_srd(blk >= 8 ? p.wdim : p.wdre, 512u * 512u * 2u), vd0, vd1, (blk & 7u) * 128u, sl);
        else dma2c(sws, vl, WS_WDH + ((unsigned)C_DN(b) * 16u + blk) * 8192u + wl * 2048, sl);
      };
      WAITV(6); BAR();
      STAMPB(30);
      wdblk(0); wdblk(1);
      WAITV(4); BAR();
      STAMP(3);
#pragma unroll 1
      for (int n = 2; n < 15; ++n) { WAITV(WD_PACE); wdblk(n); }
      lds_wait_ge(SY_R31, 4);
      wdblk(15);
    }
    const int tp4 = C_TP4(b), ft = C_FT(b), rot4 = C_ROT4(b);
    if (wv < 7) {
      bf16x8 W[32];
      WAITV(0);
      lds_set(SY_WD + wl, 1);
      STAMP(4);
      load_w<SY_PQGO, SY_PQL, 4>(W, rws, 7 * ft + wv, rot4, lane);
      STAMP(5);
      k4_body<true>(W, rws, sws, ws, wv, lane, tp4, ft, rot4);
      STAMP(8);
      lds_wait_ge(SY_WDT, 1);
      if (LATE_W) lds_wait_ge(SY_GSIG, 7);
#pragma unroll
      for (int k = 0; k < 24; ++k) {
        const int n = 19 + 3 * wl + (k >> 3);
        const int kb = (n >> 1) + 14 * (n & 1);
        W[k] = __builtin_bit_cast(bf16x8, __builtin_amdgcn_raw_buffer_load_b128(rws, vl + (k & 3) * 1024, WS_WDT + (unsigned)(C_DN(b) * 28 + kb) * 8192u + (k & 4) * 1024, 16));
      }
      SCHED();
      __builtin_amdgcn_s_setprio(3);
      lds_wait_ge(SY_PROG2 + wl, 4);
#pragma unroll
      for (int k = 0; k < 24; ++k) {
        const int sn = 16 + 3 * wl + (k >> 3);
        *(LAS bf16x8*)((unsigned)(sn >= 19 ? sn - 19 : sn) * 8192u + (k & 7) * 1024 + vl) = W[k];
      }
      CFENCE();
      if (lane == 0) lds_inc(SY_WL + 2);
      STAMP(9);
    } else {
      WAITV(0);
      lds_set(SY_WD + 3, 1);
      STAMP(4);
      {
        u32x4 mv[16];
#pragma unroll
        for (int k = 0; k < 16; ++k) mv[k] = __builtin_amdgcn_raw_buffer_load_b128(rws, vl + (k >> 2) * 1024, WS_MODP + (unsigned)((k & 3) * 3072 + 1536) * 4u, 16);
#pragma unroll
        for (int g = 0; g < 4; ++g) {
          float m[4];
#pragma unroll
          for (int j = 0; j < 4; ++j) m[j] = rbf((u2f(mv[4 * g][j]) + u2f(mv[4 * g + 1][j])) + (u2f(mv[4 * g + 2][j]) + u2f(mv[4 * g + 3][j]))) + (g < 2 ? 0.f : 1.f);
          const u32x2 o = {pk2(m[0], m[1]), pk2(m[2], m[3])};
          *(LAS u32x2*)(L_V + g * 512 + lane * 8) = o;
        }
        CFENCE();
        lds_set(SY_V, 1);
        STAMP(9);
      }
      const unsigned baseAP = lds_get(SY_BAP + (lane & 3));
      lds_wait_ge(SY_K3D, 4);
      STAMP(5);
      const int et = C_DN(b);
      auto wdn = [&](int n0, int n1) {
#pragma unroll 1
        for (int n = n0; n < n1; ++n) {
          const int kb = (n >> 1) + 14 * (n & 1);
          int sn = n + 16; sn = sn >= 38 ? sn - 38 : sn >= 19 ? sn - 19 : sn;
          const unsigned so = WS_WDT + (unsigned)(et * 28 + kb) * 8192u, sl = (unsigned)sn * 8192u;
          dma4c(sws, vl, so, sl); dma4c(sws, vl, so + 4096, sl + 4096);
          WAITV(WN_PACE);
        }
      };
      if (A_DELAY) __builtin_amdgcn_s_sleep(A_DELAY);
      cnt_wait(ws + WS_CAP + (4 * tp4 + lane) * 256, baseAP, lane, 4);
      lds_set(SY_AGO, 1);
      STAMP(6);
      auto ablk = [&](int h0, int h1) {
#pragma unroll 1
        for (int hh = h0; hh < h1; ++hh) {
          const unsigned so = WS_AP + (unsigned)(4 * tp4 + (((hh >> 1) + AROT * ft) & 3)) * 32768u + (hh & 1) * 16384, sl = (unsigned)hh * 16384u;
#pragma unroll
          for (int q = 0; q < 4; ++q) dma4c(sws, vl, so + q * 4096, sl + q * 4096);
        }
      };
      {
        const unsigned so = WS_AP + (unsigned)(4 * tp4 + ((AROT * ft) & 3)) * 32768u + 24576;
        dma4c(sws, vl, so, 24576); dma4c(sws, vl, so + 4096, 28672);
        if (lane == 0) lds_inc(SY_AREQ);
      }
      u32x2 sp[2][8];
#pragma unroll
      for (int hf = 0; hf < 2; ++hf)
#pragma unroll
        for (int e = 0; e < 8; ++e)
          sp[hf][e] = __builtin_amdgcn_raw_buffer_load_b64(rws, lane * 8 + hf * 512, WS_STATS + (unsigned)(e * 2048 + 128 * tp4) * 8u, 16);
      CFENCE();
      WAITV(16);
      if (lane == 0) lds_inc(SY_ADONE + 0);
      STAMP(7);
#pragma unroll
      for (int hf = 0; hf < 2; ++hf) {
        float mu = 0.f, m2 = 0.f;
#pragma unroll
        for (int e = 0; e < 8; ++e) { mu += u2f(sp[hf][e][0]); m2 += u2f(sp[hf][e][1]); }
        mu *= 0.125f;
        float dv = 0.f;
#pragma unroll
        for (int e = 0; e < 8; ++e) { const float d = u2f(sp[hf][e][0]) - mu; dv = fmaf(d, d, dv); }
        const float rstd = __builtin_amdgcn_rsqf(fmaf(dv, 64.f, m2) * (1.f / 512.f) + 1e-5f);
        const f32x2 o = {rstd, -mu * rstd};
        *(LAS f32x2*)(L_ST + (64 * hf + lane) * 8) = o;
      }
      WAITL0(); lds_set(SY_STAT, 1);
      STAMP(17);
      if (A_HOLD) lds_wait_ge(SY_PROG + 0, 4);
      const unsigned sc = 18 * 8192;
      ablk(2, 4);
      {
        const unsigned d0 = (unsigned)(64 * (b >> 3) + (lane >> 3)) * 1024u + (lane & 7) * 16 + (b < 224 ? 0 : 0x40000000);
        const u32x4 sd = mk_srd(p.wdn, 1792u * 512u * 2u);
#pragma unroll
        for (int k = 0; k < 4; ++k) dma2n(sd, d0 + k * 16384, d0 + k * 16384 + 8 * 1024 - 1024, (b & 7) * 128, sc + k * 2048);
      }
      WAITV(8); lds_set(SY_ADONE + 1, 1);
      STAMP(16);
      ablk(4, 6);
      WAITV(32);
      {
        const unsigned ga = sc + (8 * (lane >> 4) + ((lane & 15) >> 2)) * 128 + (lane & 3) * 8;
        const unsigned vo = vl + (b < 224 ? 0 : 0x40000000);
#pragma unroll
        for (int k = 0; k < 8; ++k) {
          const unsigned a = ga + (k >> 2) * 4096 + (k & 3) * 32;
          st128(__builtin_bit_cast(u32x4, lds_tr2(a, a + 512)), rws, vo + (k & 3) * 1024, WS_WDT + (unsigned)(((b & 7) * 28 + (b >> 3)) * 8 + (k & 4)) * 1024u, 16);
        }
      }
      WAITV(8); lds_set(SY_ADONE + 2, 1);
      STAMP(18);
      ablk(6, 8);
      WAITV(32);
      round_add(ws, 1, b, lane);
      STAMP(10);
      WAITV(1); lds_set(SY_ADONE + 3, 1);
      STAMP(19);
      cnt_wait<64, 4>(round_word(ws, 1, b, lane & 3), lds_get(SY_BAP + 8 + (lane & 3)), lane, 4);
      lds_set(SY_WDT, 1);
      STAMP(11);
      wdn(0, 3);
      lds_wait_ge(SY_UFIN + 0, 7); wdn(3, 7);
      lds_wait_ge(SY_UFIN + 1, 7); wdn(7, 11);
      WAITV(8); lds_set(SY_WL + 0, 1);
      STAMP(20);
      lds_wait_ge(SY_UFIN + 2, 7); wdn(11, 15);
      lds_wait_ge(LATE_W ? SY_GSIG : SY_UFIN + 3, 7); wdn(15, 19);
      WAITV(0); lds_set(SY_WL + 1, 1);
      STAMP(8);
      STAMP(22);
    }
  } else {
    const int w = wv;
    unsigned epoch, basePQ, baseG;
    {
      unsigned cc;
      const int b = b0;
      const unsigned lane = lane0, q = lane >> 4, r16 = lane & 15;
      u32x4 xa, xb;
      asm volatile("global_load_dwordx4 %0, %2, off nt\n\tglobal_load_dwordx4 %1, %2, off offset:1024 nt" : "=&v"(xa), "=&v"(xb) : "v"((const char*)p.x + (8 * b + 2 * w) * 1024 + lane * 16) : "memory");
      unsigned sv = 0, ev;
      asm volatile("global_load_dword %0, %1, off" : "=&v"(cc) : "v"((const char*)p.c + (b & 3) * 256 + lane * 4) : "memory");
      asm volatile("global_load_dword %0, %1, off sc1" : "=&v"(ev) : "v"(ws + WS_NONCE) : "memory");
      {
        const unsigned off = lane < 2 ? (lane ? WS_CG : WS_CPQ) + (2 * C_TB64(b) + (w >> 1)) * 256 + HALFC * (w & 1) * 128
                             : lane < 6 ? WS_CAP + (4 * C_TP4(b) + lane - 2) * 256 : WS_RND + ((lane - 6) >> 2) * 8192 + ((b >> 3) & 15) * 512 + ((lane - 6) & 3) * 128;
        if (lane < (w == 0 ? 14u : 2u)) asm volatile("global_atomic_add %0, %1, %2, off sc0" : "=&v"(sv) : "v"(ws + off), "v"(0u) : "memory");
      }
      if (threadIdx.x < SY_N) lds_set(threadIdx.x, 0);
      WAITL0(); BAR();
      __builtin_amdgcn_s_setprio(1);
      asm volatile("s_waitcnt vmcnt(2)" : "+v"(xa), "+v"(xb), "+v"(cc)::"memory");
      STAMP(13);
      {
        const bf2 one = __builtin_bit_cast(bf2, 0x3f803f80u);
        float sa = 0.f, sb = 0.f, qa = 0.f, qb = 0.f;
#pragma unroll
        for (int i = 0; i < 4; ++i) {
          const unsigned ua = xa[i], ub = xb[i];
          const bf2 pa = __builtin_bit_cast(bf2, ua), pb = __builtin_bit_cast(bf2, ub);
          sa = __builtin_amdgcn_fdot2_f32_bf16(pa, one, sa, false); qa = __builtin_amdgcn_fdot2_f32_bf16(pa, pa, qa, false);
          sb = __builtin_amdgcn_fdot2_f32_bf16(pb, one, sb, false); qb = __builtin_amdgcn_fdot2_f32_bf16(pb, pb, qb, false);
        }
        sa = wave_sum(sa) * (1.f / 512.f); sb = wave_sum(sb) * (1.f / 512.f);
        qa = wave_sum(qa) * (1.f / 512.f); qb = wave_sum(qb) * (1.f / 512.f);
        qa = __builtin_amdgcn_rsqf(__builtin_fmaxf(fmaf(-sa, sa, qa), 0.f) + 1e-5f); qb = __builtin_amdgcn_rsqf(__builtin_fmaxf(fmaf(-sb, sb, qb), 0.f) + 1e-5f);
        sa *= -qa; sb *= -qb;
        u32x4 na, nb;
#pragma unroll
        for (int i = 0; i < 4; ++i) {
          na[i] = pk2(fmaf(bflo(xa[i]), qa, sa), fmaf(bfhi(xa[i]), qa, sa));
          nb[i] = pk2(fmaf(bflo(xb[i]), qb, sb), fmaf(bfhi(xb[i]), qb, sb));
        }
        const unsigned lr = L_LN + (w >> 1) * L_STAGE + (w & 1) * 2176 + lane * 16;
        *(LAS u32x4*)lr = na;
        *(LAS u32x4*)(lr + 1088) = nb;
      }
      CFENCE();
      if (lane == 0) lds_inc(SY_LN);
      lds_wait_ge<0>(SY_LN, 4);
      {
        const unsigned la = L_LN + (r16 >> 2) * 1088 + (128 * w + 16 * q + 4 * (lane & 3)) * 2;
#pragma unroll
        for (int i = 0; i < 2; ++i) {
          const bf16x8 f = lds_tr2(la + i * 128, la + i * 128 + L_STAGE);
          st128(__builtin_bit_cast(u32x4, f), rws, lane * 16, WS_NT + (unsigned)((2 * w + i) * 256 + b) * 1024u, 16);
        }
      }
      {
        const float c0 = bflo(cc), c1 = bfhi(cc);
        *(LAS unsigned*)(L_HK + w * 256 + lane * 4) =
            pk2(c0 * __builtin_amdgcn_rcpf(1.f + __builtin_amdgcn_exp2f(c0 * -1.44269504f)), c1 * __builtin_amdgcn_rcpf(1.f + __builtin_amdgcn_exp2f(c1 * -1.44269504f)));
        CFENCE();
      }
      asm volatile("s_waitcnt vmcnt(0)" : "+v"(sv), "+v"(ev)::"memory");
      basePQ = __builtin_amdgcn_readlane(sv, 0); baseG = __builtin_amdgcn_readlane(sv, 1);
      epoch = RFL(ev) + 1u;
      unsigned old = 0;
      if (lane == 0) old = lds_inc(SY_ND);
      if (RFL(old) == 3) first_set(ws, b, lane, epoch);
      STAMP(1);
      if (w == 0 && lane >= 2 && lane < 14) lds_set(SY_BAP + lane - 2, sv);
      const char* fp = ws + WS_FLAG + ((b >> 3) & 15) * 1024 + (w * 64 + lane) * 4;
      u64 seen = 0;
      const u64 first = FINE ? 0xffull << (2 * C_J0(b)) : ~0ull;
_Pragma("unroll 1") for (int sp = 0; sp < SPIN_CAP; ++sp) {
        seen = __builtin_amdgcn_ballot_w64(atomic_peek(fp) == epoch);
        if ((seen & first) == first) break;
      }
      lds_wait_ge<0>(SY_ND, 4);
#pragma unroll
      for (int s = 0; s < 4; ++s) dma2c(sws, lane * 16, WS_NT + (unsigned)(C_DN(b) * 32 + C_J0(b) + s) * 8192u + w * 2048, s * L_STAGE + 16384 + w * 2048);
      STAMP(2);
_Pragma("unroll 1") for (int sp = 0; sp < SPIN_CAP && seen != ~0ull; ++sp) seen = __builtin_amdgcn_ballot_w64(atomic_peek(fp) == epoch);
      if (lane == 0) lds_inc(SY_SEEN);
    }

    {
      f32x4 acc[2][4] = {}, rsum[2] = {};
      const unsigned o1 = (lane0 & 3) == 0 ? 0x3f803f80u : 0u;
      const u32x4 ones4 = {o1, o1, o1, o1};
      const bf16x8 sel = __builtin_bit_cast(bf16x8, ones4);
      bf16x8 N[2][8], Wf[2][4];
      const unsigned nb0 = (lane0 >> 4) * 1024 + (lane0 & 15) * 16 + 16384;
      const unsigned wa0 = (32 * w + (lane0 & 15)) * 128 + (((lane0 >> 4) ^ ((lane0 >> 1) & 7)) * 16);
      const unsigned nbA = opv(nb0), nbB = opv(nb0 + 2 * L_STAGE), waA = opv(wa0), waB = opv(wa0 + 2 * L_STAGE), wbA = opv(wa0 ^ 64), wbB = opv((wa0 ^ 64) + 2 * L_STAGE);
      auto rd1 = [&](int s, int i) {
        const unsigned o = (s & 1) * L_STAGE;
        const unsigned nb = (s & 2) ? nbB : nbA, wa = (s & 2) ? waB : waA, wb = (s & 2) ? wbB : wbA;
        const int kk = i / 6, j = i % 6;
        if (j < 2) Wf[s & 1][2 * kk + j] = lds_frag((kk ? wb : wa) + o + j * 2048);
        else N[s & 1][4 * kk + j - 2] = lds_frag(nb + o + kk * 4096 + (j - 2) * 256);
      };
      auto mm1 = [&](int s, int i) {
        const int kk = i / 10, j = i % 10;
        if (j < 8) acc[j & 1][j >> 1] = MFMA16(N[s & 1][4 * kk + (j >> 1)], Wf[s & 1][2 * kk + (j & 1)], acc[j & 1][j >> 1]);
        else rsum[j & 1] = MFMA16(sel, Wf[s & 1][2 * kk + (j & 1)], rsum[j & 1]);
      };
      unsigned pk1 = 0, pb1 = 0;
      WAITV(6); BAR();
      STAMP(3);
      static_for<0, 12>([&](auto ic) { rd1(0, decltype(ic)::value); });
      WAITL0();
      WAITV(4);
      static_for<1, 32>([&](auto sc) {
        constexpr int s = decltype(sc)::value;
        SCHED(); BAR(); SCHED();
        static_for<0, 20>([&](auto ic) {
          constexpr int i = decltype(ic)::value;
          mm1(s - 1, i);
          SCHED();
          if constexpr (i < 12) rd1(s, i);
          SCHED();
        });
        asm volatile("" : "+v"(acc[0][0]), "+v"(acc[0][1]), "+v"(acc[0][2]), "+v"(acc[0][3]), "+v"(acc[1][0]), "+v"(acc[1][1]), "+v"(acc[1][2]), "+v"(acc[1][3]), "+v"(rsum[0]), "+v"(rsum[1]));
        WAITL0();
        SCHED();
        if constexpr (s == 1) {
          WAITV(2);
        } else if constexpr (s == 2) {
          WAITV(0);
        } else if constexpr (s == 5) {
          if (w == 0) {
            round_add(ws, 0, ops(b0), opv(lane0));
            if (ops(b0) == 0 && lane0 == 0) flag_st(ws + WS_NONCE, epoch);
          }
        } else if constexpr (s == HC) {
          if (w == 1) {
            const unsigned lane = opv(lane0);
            pb1 = lds_get(SY_BAP + 4 + (lane & 3));
            if (lane < 4) asm volatile("global_atomic_add %0, %1, %2, off sc0" : "=&v"(pk1) : "v"(round_word(ws, 0, ops(b0), lane)), "v"(0u) : "memory");
          }
        } else if constexpr (s == HC + 2) {
          if (w == 1) {
            const unsigned lane = opv(lane0);
            WAITV(0);
            asm volatile("" : "+v"(pk1));
            if (!all_lanes(lane >= 4 || pk1 - pb1 >= 64u)) cnt_wait<64, 8>(round_word(ws, 0, ops(b0), lane & 3), pb1, lane, 4);
          }
        } else if constexpr (s == HC + 3) {
          const int b = ops(b0), dn = C_DN(b);
          const unsigned lane = opv(lane0);
          const unsigned mo = (lane >> 4) * 12288 + (lane & 15) * 16;
          dma1c(sws, mo, WS_MODP + (unsigned)((w == 3 ? 32 : 8 * w) + dn) * 256u, L_AUX + w * 2048);
          dma1c(sws, mo, WS_MODP + (unsigned)(40 + dn) * 256u, L_AUX + w * 2048 + 1024);
          if (!XPRO) {
            const unsigned x0 = (unsigned)(64 * C_TB64(b) + 16 * w + (lane >> 3)) * 1024u + ((lane & 7) ^ (lane >> 3)) * 16;
            dma2v(mk_srd(p.x, 2048u * 512u * 2u), x0, x0 + 8 * 1024 - 1024, dn * 128, L_X + w * 2048);
          }
        } else if constexpr (s == HC + 5) {
          WAITV(0);
        } else if constexpr (s == 31) {
          if (lane0 == 0) lds_inc(SY_R31);
        }
      });
      const unsigned lane = opv(lane0), q = lane >> 4, r16 = lane & 15;
      const unsigned tb = opv(L_MOD + q * 16);
      f32x4 sa[4], c1[4];
#pragma unroll
      for (int i = 0; i < 4; ++i) { sa[i] = lds_f4(tb + i * 64); c1[i] = lds_f4(tb + 256 + i * 64); }
      SCHED();
      const int b = ops(b0);
      const unsigned vo = ((q & 1) * 2 + (q >> 1)) * 256 + r16 * 16 + WS_PQ + (unsigned)((8 * C_TP(b) + 2 * w) * 32 + C_PART(b) * 16 + 2 * C_DN(b)) * 1024u;
      static_for<0, 10>([&](auto ic) { mm1(31, 2 * decltype(ic)::value); });
      SCHED();
      static_for<0, 10>([&](auto ic) { mm1(31, 2 * decltype(ic)::value + 1); });
      STAMP(4);
#pragma unroll
      for (int tt = 0; tt < 2; ++tt) {
        const float rs = rsum[tt][0];
        unsigned pk[8];
#pragma unroll
        for (int i = 0; i < 4; ++i) {
          pk[2 * i] = pk2(fmaf(acc[tt][i][0], c1[i][0], rs * sa[i][0]), fmaf(acc[tt][i][1], c1[i][1], rs * sa[i][1]));
          pk[2 * i + 1] = pk2(fmaf(acc[tt][i][2], c1[i][2], rs * sa[i][2]), fmaf(acc[tt][i][3], c1[i][3], rs * sa[i][3]));
        }
#pragma unroll
        for (int pr = 0; pr < 2; ++pr)
          st128(merge16(pk[4 * pr], pk[4 * pr + 1], pk[4 * pr + 2], pk[4 * pr + 3]), rws, vo, pr * 1024 + tt * 32768, 16);
        if (tt == 0) {
#pragma unroll
          for (int i = 0; i < 10; ++i) { SGB(0x8, 1); SGB(0x2, 4); }
          SCHED();
        }
      }
      WAITV(0);
      if (lane == 0) cnt_add(ws + WS_CPQ + (4 * C_TP(b) + w) * 256 + HALFC * C_PART(b) * 128);
      STAMP(5);
    }

    float x1[16];
    {
      const int b = ops(b0);
      const unsigned lane = opv(lane0), q = lane >> 4, r16 = lane & 15, vl = lane * 16;
      const int tb64 = C_TB64(b), et = C_DN(b), tb16 = 4 * tb64 + w, t32c = 2 * tb64 + (w >> 1);
      lds_wait4(SY_WD);
      STAMP(15);
      cnt_wait<HALFC ? 8 : 16>(ws + WS_CPQ + t32c * 256 + HALFC * (w & 1) * 128, basePQ, lane, 1);
      lds_set(SY_PQGO, 1);
      STAMP(6);
      const int kh = w & 1;
      bf16x8 pq[32];
      auto ldp = [&](int n) {
        pq[n] = __builtin_bit_cast(bf16x8, __builtin_amdgcn_raw_buffer_load_b128(rws, vl + ((n >> 1) & 1) * 1024, WS_PQ + (unsigned)((2 * t32c + (n & 1)) * 32 + 16 * kh + (n >> 2) * 2) * 1024u, 16));
      };
      static_for<0, P_WIN>([&](auto nc) { ldp(decltype(nc)::value); });
      SCHED();
      f32x4 y2[2][4] = {};
      bf16x8 F[64];
      unsigned fa, fb;
      const unsigned la = r16 * 128 + ((q ^ (r16 >> 1)) * 16);
      auto rdw = [&](int i) {
        if ((i & 7) == 0) {
          int nl = ((i >> 3) + 8 * kh - WDROT * 4 * (tb64 & 3)) & 15;
          nl = nl < 6 ? nl + 12 : nl < 15 ? nl - 6 : 11;
          if (RAWWD) { fa = opv(la + (unsigned)nl * 8192u); fb = opv((la ^ 64u) + (unsigned)nl * 8192u); }
          else fa = opv(vl + (unsigned)nl * 8192u);
        }
        if (RAWWD) F[i] = lds_frag(((i & 4) ? fb : fa) + (i & 3) * 2048);
        else F[i] = lds_frag(fa + (i & 7) * 1024);
      };
      static_for<0, 6>([&](auto ic) { rdw(decltype(ic)::value); });
      SCHED();
      static_for<0, 64>([&](auto ic) {
        constexpr int i = decltype(ic)::value;
        y2[0][i & 3] = MFMA16(F[i], pq[2 * (i >> 2)], y2[0][i & 3]);
        SCHED();
        if constexpr (i + 6 < 64) rdw(i + 6);
        SCHED();
        y2[1][i & 3] = MFMA16(F[i], pq[2 * (i >> 2) + 1], y2[1][i & 3]);
        SCHED();
        if constexpr ((i & 1) == 0 && (i >> 1) + P_WIN < 32) ldp((i >> 1) + P_WIN);
        if constexpr (i == PQ_SIG) { if (lane == 0) lds_inc(SY_PQL); }
        if constexpr (i == 3) STAMP(28);
        if constexpr (i == 31) STAMP(29);
        if constexpr (i == 63) STAMP(30);
        SCHED();
      });
      const unsigned tb = opv(L_MOD + 512 + q * 16);
      f32x4 ga[4], c1[4];
      u32x2 xr[4];
#pragma unroll
      for (int e = 0; e < 4; ++e) {
        ga[e] = lds_f4(tb + e * 64); c1[e] = lds_f4(tb + 256 + e * 64);
        xr[e] = *(const LAS u32x2*)(L_X + w * 2048 + r16 * 128 + (((2 * e + (lane >> 5)) ^ (r16 & 7)) * 16) + (q & 1) * 8);
      }
      f32x4 y[4];
      pair_reduce(y, y2, 9 * 8192, SY_X3, w, lane);
      STAMP(7);
      unsigned pk[8];
      float sm = 0.f;
#pragma unroll
      for (int e = 0; e < 4; ++e) {
        x1[4 * e] = fmaf(ga[e][0], y[e][0], bflo(xr[e][0])); x1[4 * e + 1] = fmaf(ga[e][1], y[e][1], bfhi(xr[e][0]));
        x1[4 * e + 2] = fmaf(ga[e][2], y[e][2], bflo(xr[e][1])); x1[4 * e + 3] = fmaf(ga[e][3], y[e][3], bfhi(xr[e][1]));
        pk[2 * e] = pk2(x1[4 * e] * c1[e][0], x1[4 * e + 1] * c1[e][1]); pk[2 * e + 1] = pk2(x1[4 * e + 2] * c1[e][2], x1[4 * e + 3] * c1[e][3]);
        sm += (x1[4 * e] + x1[4 * e + 1]) + (x1[4 * e + 2] + x1[4 * e + 3]);
      }
      const unsigned vo = ((q & 1) * 2 + (q >> 1)) * 256 + r16 * 16;
#pragma unroll
      for (int pr = 0; pr < 2; ++pr)
        st128(merge16(pk[4 * pr], pk[4 * pr + 1], pk[4 * pr + 2], pk[4 * pr + 3]), rws, vo + pr * 1024, WS_AP + (unsigned)(tb16 * 16 + 2 * et) * 1024u, 16);
      float mu = sm * (1.f / 16.f), m2 = 0.f;
#pragma unroll
      for (int i = 0; i < 16; ++i) { const float d = x1[i] - mu; m2 = fmaf(d, d, m2); }
      {
        auto a = __builtin_amdgcn_permlane16_swap(f2u(mu), f2u(mu), false, false);
        const float d = u2f(a[0]) - u2f(a[1]);
        mu = 0.5f * (u2f(a[0]) + u2f(a[1])); m2 = fmaf(8.f * d, d, sum16(m2));
        auto c = __builtin_amdgcn_permlane32_swap(f2u(mu), f2u(mu), false, false);
        const float d2 = u2f(c[0]) - u2f(c[1]);
        mu = 0.5f * (u2f(c[0]) + u2f(c[1])); m2 = fmaf(16.f * d2, d2, sum32(m2));
      }
      if (lane < 16) {
        const u32x2 o = {f2u(mu), f2u(m2)};
        __builtin_amdgcn_raw_buffer_store_b64(o, rws, lane * 8, WS_STATS + (unsigned)(et * 2048 + 16 * tb16) * 8u, 16);
      }
      if (PQ_SIG > 63 && lane == 0) lds_inc(SY_PQL);
      if (lane == 0) lds_inc(SY_K3D);
      WAITV(0);
      if (lane == 0) cnt_add(ws + WS_CAP + t32c * 256);
      STAMP(8);
    }
    __builtin_amdgcn_s_setprio(0);
    {
      const int b = ops(b0);
      const unsigned lane = opv(lane0);
      bf16x8 W[32];
      load_w<SY_AGO, Y_DONE, 4>(W, rws, 7 * C_FT(b) + w, C_ROT4(b), lane);
      if (lane == 0) lds_inc(SY_PROG + 0);
      k4_body<false>(W, rws, sws, ws, w, lane, C_TP4(b), C_FT(b), C_ROT4(b));
    }
    STAMP(9);

    {
      const int b = ops(b0);
      const unsigned lane = opv(lane0), q = lane >> 4, r16 = lane & 15, vl = lane * 16;
      const int tb64 = C_TB64(b), et = C_DN(b), tb16 = 4 * tb64 + w, t32c = 2 * tb64 + (w >> 1);
      cnt_wait<HALFC ? 8 : 16>(ws + WS_CG + t32c * 256 + HALFC * (w & 1) * 128, baseG, lane, 1);
      STAMP(10);
      const int kh = w & 1;
      const unsigned gb0 = WS_G + (unsigned)(2 * t32c) * 57344u + (unsigned)kh * 28672u;
      const unsigned vy = opv(vl + 57344);
      bf16x8 g[56];
      auto ldg = [&](int n) {
        g[n] = __builtin_bit_cast(bf16x8, __builtin_amdgcn_raw_buffer_load_b128(rws, ((n & 1) ? vy : vl) + ((n >> 1) & 3) * 1024, gb0 + (unsigned)(n >> 3) * 4096u, 16));
      };
      static_for<0, G_WIN>([&](auto nc) { ldg(decltype(nc)::value); });
      f32x4 y2[2][4] = {};
      bf16x8 F[112];
      unsigned fa;
      auto rdw = [&](int i) {
        if ((i & 7) == 0) {
          const int m = i >> 3;
          if (m == 5) lds_wait_ge(SY_WL + 1, 1);
          if (m == 9) lds_wait_ge(SY_WL + 2, 3);
          const int s0 = (16 + 2 * m) % 19, s1 = (17 + 2 * m) % 19;
          fa = opv(vl + (unsigned)(kh ? s1 : s0) * 8192u);
        }
        F[i] = lds_frag(fa + (i & 7) * 1024);
      };
      lds_wait_ge(SY_WL + 0, 1);
      static_for<0, 6>([&](auto ic) { rdw(decltype(ic)::value); });
      SCHED();
      static_for<0, 112>([&](auto ic) {
        constexpr int i = decltype(ic)::value;
        y2[0][i & 3] = MFMA16(F[i], g[2 * (i >> 2)], y2[0][i & 3]);
        SCHED();
        if constexpr (i + 6 < 112) rdw(i + 6);
        SCHED();
        y2[1][i & 3] = MFMA16(F[i], g[2 * (i >> 2) + 1], y2[1][i & 3]);
        SCHED();
        if constexpr ((i & 1) == 0 && (i >> 1) + G_WIN < 56) ldg((i >> 1) + G_WIN);
        if constexpr (i == 15 || i == 23 || i == 39) { if (lane == 0) lds_inc(SY_PROG2 + (i >> 4)); }
        if constexpr (i == 47) { if (lane == 0) lds_inc(SY_PROG + 1); }
        if constexpr (i == 96) lds_wait_ge(SY_PROG + 1, 4);
        if constexpr (i == 3) STAMP(24);
        if constexpr (i == 39) STAMP(25);
        if constexpr (i == 79) STAMP(26);
        if constexpr (i == 111) STAMP(27);
        SCHED();
      });
      const unsigned tb = opv(L_MOD + 1024 + q * 16);
      f32x4 gb[4], y[4];
#pragma unroll
      for (int e = 0; e < 4; ++e) gb[e] = lds_f4(tb + e * 64);
      pair_reduce(y, y2, 6 * 8192, SY_X5, w, lane);
      STAMP(11);
      unsigned pk[8];
#pragma unroll
      for (int e = 0; e < 4; ++e) {
        pk[2 * e] = pk2(fmaf(gb[e][0], y[e][0], x1[4 * e]), fmaf(gb[e][1], y[e][1], x1[4 * e + 1]));
        pk[2 * e + 1] = pk2(fmaf(gb[e][2], y[e][2], x1[4 * e + 2]), fmaf(gb[e][3], y[e][3], x1[4 * e + 3]));
      }
      const __amdgpu_buffer_rsrc_t ro = mk_rsrc(p.out, 2048u * 512u * 2u);
#pragma unroll
      for (int pr = 0; pr < 2; ++pr)
        st128(merge16(pk[4 * pr], pk[4 * pr + 1], pk[4 * pr + 2], pk[4 * pr + 3]), ro, r16 * 1024 + (q & 1) * 32 + (q >> 1) * 16 + pr * 64,
                                               (unsigned)(16 * tb16) * 1024u + et * 128, 16);
    }
    STAMP(12);
  }
}

#if defined(__HIP_DEVICE_COMPILE__)
#pragma clang attribute pop
#endif

extern "C" void kernel_launch(void* const* d_in, const int* in_sizes, int n_in, void* d_out, int out_size, void* d_ws, size_t ws_size,
                              hipStream_t stream) {
  Params p;
  p.x = (const u16*)d_in[0]; p.wsre = (const u16*)d_in[1]; p.wsim = (const u16*)d_in[2];
  p.wdre = (const u16*)d_in[3]; p.wdim = (const u16*)d_in[4];
  p.wg = (const u16*)d_in[6]; p.wu = (const u16*)d_in[7]; p.wdn = (const u16*)d_in[8];
  p.c = (const u16*)d_in[9]; p.wmod = (const u16*)d_in[10];
  p.out = (u16*)d_out; p.ws = (char*)d_ws;
  (void)hipFuncSetAttribute((const void*)fused_block, hipFuncAttributeMaxDynamicSharedMemorySize, LDS_BYTES);
  fused_block<<<256, 512, LDS_BYTES, stream>>>(p);
}
```
